```python
import torch

def _softmax(x, axis=-1):
    return torch.softmax(x, dim=axis)
def _gelu(x):
    return torch.nn.functional.gelu(x, approximate="tanh")
def _silu(x):
    return torch.nn.functional.silu(x)
def _sigmoid(x):
    return torch.sigmoid(x)
def _layernorm(x, axis=-1, eps=1e-5):
    m = x.mean(dim=axis, keepdim=True)
    v = x.var(dim=axis, keepdim=True, unbiased=False)
    return (x - m) / torch.sqrt(v + eps)
def _scatter_add(tgt, idx, src, axis=0):
    return tgt.clone().index_add_(axis, idx, src)
def _segment_sum(src, seg, n):
    out = torch.zeros((n,) + src.shape[1:], dtype=src.dtype)
    return out.index_add_(0, seg, src)
def _pad(x, pw):
    flat = tuple(v for pair in reversed(pw) for v in pair)
    return torch.nn.functional.pad(x, flat)

kernel_name = 'sequential_preLN__spectral_gfnet__moe_arch_mod__prompt'

def setup_inputs():

    _w = (lambda a: (lambda t: t.to(torch.bfloat16) if t.is_floating_point() else t)(torch.as_tensor(a)))
    def _A_setup():
        B, S, D = 1, 2048, 1024
        Df = D // 2 + 1
        return {
            "x": _w((lambda *s: torch.randn(*s, dtype=torch.bfloat16) * 0.05)(B, S, D)),
            "w_r": _w((lambda *s: torch.randn(*s, dtype=torch.bfloat16) * 1.6)(S, Df) * 0.02),
            "w_i": _w((lambda *s: torch.randn(*s, dtype=torch.bfloat16) * 1.6)(S, Df) * 0.02),
        }
    _A_in = _A_setup()

    def _B_setup():
        B, S, D, F, C = 1, 2048, 1024, 3584, 8
        return {
            "x": _w((lambda *s: torch.randn(*s, dtype=torch.bfloat16) * 0.05)(B, S, D)),
            "w_r": _w((lambda *s: torch.randn(*s, dtype=torch.bfloat16) * 0.2)(D)),
            "w_gate": _w((lambda *s: torch.randn(*s, dtype=torch.bfloat16) * 0.2)(D, F)),
            "w_up": _w((lambda *s: torch.randn(*s, dtype=torch.bfloat16) * 0.2)(D, F)),
            "w_down": _w((lambda *s: torch.randn(*s, dtype=torch.bfloat16) * 0.2)(F, D)),
        }
    _B_in = _B_setup()

    return {
        "A_x": _A_in["x"],
        "A_w_r": _A_in["w_r"],
        "A_w_i": _A_in["w_i"],
        "B_x": _B_in["x"],
        "B_w_r": _B_in["w_r"],
        "B_w_gate": _B_in["w_gate"],
        "B_w_up": _B_in["w_up"],
        "B_w_down": _B_in["w_down"],
    }

def reference(A_x, A_w_r, A_w_i, B_x, B_w_r, B_w_gate, B_w_up, B_w_down):
    def _A_ref(x, w_r, w_i):
        B, S, D = x.shape
        X = (lambda x, axes=(-2,-1): torch.fft.rfft2(x.float(), dim=axes))(x, axes=(-2, -1))
        W = w_r + 1j * w_i
        X = X * W
        y = (lambda x, s, axes=(-2,-1): torch.fft.irfft2(x, s=s, dim=axes).to(torch.bfloat16))(X, (S, D), axes=(-2, -1))
        return y

    def _B_ref(x, w_r, w_gate, w_up, w_down):
        B, S, D = x.shape
        C = 8
        r = x @ w_r
        idx = torch.argsort(-r, axis=-1)[:, :C]
        idx_e = (lambda x, n, axis: torch.repeat_interleave(x, n, dim=axis))(idx.reshape(B, C, 1), D, axis=2)
        x_sel = (lambda x, i, axis: torch.take_along_dim(x, i, dim=axis))(x, idx_e, axis=1)
        g = _silu(x_sel @ w_gate)
        u = x_sel @ w_up
        y = (g * u) @ w_down
        r_sel = _sigmoid((lambda x, i, axis: torch.take_along_dim(x, i, dim=axis))(r, idx, axis=1))
        y = y * r_sel.reshape(B, C, 1)
        b_off = (torch.arange(B) * S).reshape(B, 1)
        flat_idx = (idx + b_off).reshape(B * C)
        zero = (x * 0.0).reshape(B * S, D)
        upd = _scatter_add(zero, flat_idx, y.reshape(B * C, D), axis=0)
        return x + upd.reshape(B, S, D)

    x = A_x
    h = x + _A_ref(_layernorm(x, axis=-1), A_w_r, A_w_i)
    h2 = h + _B_ref(_layernorm(h, axis=-1), B_w_r, B_w_gate, B_w_up, B_w_down)
    return h2
```

```cpp
#include <hip/hip_runtime.h>
#include <stdint.h>

#define GAS __attribute__((address_space(1)))
#define LAS __attribute__((address_space(3)))
typedef _Float16 h2 __attribute__((ext_vector_type(2)));
typedef _Float16 h8 __attribute__((ext_vector_type(8)));
typedef __bf16 b2 __attribute__((ext_vector_type(2)));
typedef __bf16 b8 __attribute__((ext_vector_type(8)));
typedef float f4 __attribute__((ext_vector_type(4)));
typedef unsigned u2 __attribute__((ext_vector_type(2)));
typedef unsigned u4 __attribute__((ext_vector_type(4)));
typedef short s4 __attribute__((ext_vector_type(4)));
typedef unsigned short us;

#define DEV __device__ __forceinline__

constexpr int S_ = 2048, D_ = 1024, DF = 513, F_ = 3584;

constexpr size_t WS_G1 = 0;
constexpr size_t WS_G2 = 257 * 16384;
constexpr size_t WS_LN2 = 2 * 257 * 16384;
constexpr size_t WS_H = WS_LN2 + 4194304;
constexpr size_t WS_KEY = WS_H + 4194304;
constexpr size_t WS_ACT = WS_KEY + 8192;
constexpr size_t WS_SEL = WS_ACT + 65536;
constexpr size_t WS_PART = WS_SEL + 256;

DEV unsigned pkh(float a, float b) { h2 v = {(_Float16)a, (_Float16)b}; return __builtin_bit_cast(unsigned, v); }
DEV unsigned pkb(float a, float b) { b2 v = {(__bf16)a, (__bf16)b}; return __builtin_bit_cast(unsigned, v); }
DEV float asf(unsigned u) { return __builtin_bit_cast(float, u); }
DEV float blo(unsigned u) { return __builtin_bit_cast(float, u << 16); }
DEV float bhi(unsigned u) { return __builtin_bit_cast(float, u & 0xffff0000u); }
DEV float rbf(float a) { return blo(pkb(a, a)); }
DEV f4 mmh(u4 a, u4 b, f4 c) { return __builtin_amdgcn_mfma_f32_16x16x32_f16(__builtin_bit_cast(h8, a), __builtin_bit_cast(h8, b), c, 0, 0, 0); }
DEV f4 mmb(u4 a, u4 b, f4 c) { return __builtin_amdgcn_mfma_f32_16x16x32_bf16(__builtin_bit_cast(b8, a), __builtin_bit_cast(b8, b), c, 0, 0, 0); }
DEV float cosr(float rev) { return __builtin_amdgcn_cosf(rev); }
DEV float sinr(float rev) { return __builtin_amdgcn_sinf(rev); }
DEV float dot2b(unsigned a, unsigned b, float c) { return __builtin_amdgcn_fdot2_f32_bf16(__builtin_bit_cast(b2, a), __builtin_bit_cast(b2, b), c, false); }

template <int CTRL> DEV float dppf(float x) { return __builtin_bit_cast(float, __builtin_amdgcn_update_dpp(0, __builtin_bit_cast(int, x), CTRL, 0xf, 0xf, true)); }
DEV float xrow_sum(float x) {
  unsigned u = __builtin_bit_cast(unsigned, x);
  auto a = __builtin_amdgcn_permlane16_swap(u, u, false, false);
  x = asf(a[0]) + asf(a[1]);
  u = __builtin_bit_cast(unsigned, x);
  auto b = __builtin_amdgcn_permlane32_swap(u, u, false, false);
  return asf(b[0]) + asf(b[1]);
}
DEV float sum_parity(float x) {
  x += dppf<0x4E>(x);
  x += dppf<0x124>(x);
  x += dppf<0x128>(x);
  return xrow_sum(x);
}
DEV float sum_wave(float x) {
  x += dppf<0xB1>(x);
  return sum_parity(x);
}

DEV void ln_scalars(float mean, float var, float& mb, float& rs) {
  mb = rbf(mean);
  float vb = rbf(var);
  float sd = rbf(__builtin_amdgcn_sqrtf(rbf(vb + 1e-5f)));
  rs = 1.0f / sd;
}

DEV int pos_of(int sp) { return (sp & 15) * 16 + ((sp >> 4) & 3) * 4 + (sp >> 6); }

constexpr int ZSTR = 1026;

__global__ __launch_bounds__(256, 2) void k1_rows_fwd(const us* __restrict__ x, unsigned* __restrict__ G1) {
  __shared__ __attribute__((aligned(16))) unsigned zs[4 * ZSTR];
  const int sp = blockIdx.x, tid = threadIdx.x, l = tid & 63, w = __builtin_amdgcn_readfirstlane(tid >> 6);
  const int r = l & 15, g = l >> 4;
  const us* row = x + (size_t)(sp + 256 * (w + 4 * (r & 1))) * D_ + 256 * g + 4 * (r >> 1);
  u2 v[8];
#pragma unroll
  for (int j = 0; j < 8; ++j) v[j] = *(const u2*)(row + 32 * j);

  u4 Cm[2], Sm[2], Nm[2];
  float twc[2][8], tws[2][8];
#pragma unroll
  for (int t = 0; t < 2; ++t) {
    float c[8], s[8];
#pragma unroll
    for (int j = 0; j < 8; ++j) {
      const int p = (8 * g + j) * (16 * t + r);
      const float a = (float)(p & 31) * (1.0f / 32.0f);
      c[j] = cosr(a); s[j] = sinr(a);
      const float b = (float)p * (1.0f / 1024.0f);
      twc[t][j] = cosr(b) * 0.125f; tws[t][j] = sinr(b) * 0.125f;
    }
#pragma unroll
    for (int q = 0; q < 4; ++q) {
      Cm[t][q] = pkh(c[2 * q], c[2 * q + 1]);
      Sm[t][q] = pkh(s[2 * q], s[2 * q + 1]);
      Nm[t][q] = Sm[t][q] ^ 0x80008000u;
    }
  }
  u4 MA, MB;
  {
    const int rho = r >> 1, part = r & 1;
    const int base = (sp + 256 * g) * rho;
    const int e = (rho & 1) ? -256 : 256;
    const float aa = (float)((base + e) & 2047) * (1.0f / 2048.0f), ab = (float)((base - e) & 2047) * (1.0f / 2048.0f);
    const float sc = 0.70710678118f / 16.0f;
    const float ar = cosr(aa) * sc, ai = -sinr(aa) * sc, br = cosr(ab) * sc, bi = -sinr(ab) * sc;
    const unsigned a0 = part ? pkh(ai, ar) : pkh(ar, -ai);
    const unsigned a1 = part ? pkh(bi, -br) : pkh(br, bi);
    MA = u4{a0, 0u, 0u, a1};
    MB = u4{0u, a0, a1, 0u};
  }

  float s1 = 0.f, s2 = 0.f;
#pragma unroll
  for (int j = 0; j < 8; ++j) {
    s1 = dot2b(v[j][0], 0x3f803f80u, s1); s1 = dot2b(v[j][1], 0x3f803f80u, s1);
    s2 = dot2b(v[j][0], v[j][0], s2); s2 = dot2b(v[j][1], v[j][1], s2);
  }
  s1 = sum_parity(s1); s2 = sum_parity(s2);
  const float mean = s1 * (1.0f / 1024.0f);
  float var = s2 * (1.0f / 1024.0f) - mean * mean;
  if (__builtin_amdgcn_ballot_w64(mean * mean > 16.0f * var)) {
    float q = 0.f;
#pragma unroll
    for (int j = 0; j < 8; ++j) {
      float d;
      d = blo(v[j][0]) - mean; q += d * d; d = bhi(v[j][0]) - mean; q += d * d;
      d = blo(v[j][1]) - mean; q += d * d; d = bhi(v[j][1]) - mean; q += d * d;
    }
    var = sum_parity(q) * (1.0f / 1024.0f);
  }
  float mb, rs;
  ln_scalars(mean, var, mb, rs);
  const float off = -mb * rs;
  u4 A[4];
#pragma unroll
  for (int q = 0; q < 4; ++q) {
    A[0][q] = pkh(blo(v[2 * q][0]) * rs + off, blo(v[2 * q + 1][0]) * rs + off);
    A[1][q] = pkh(bhi(v[2 * q][0]) * rs + off, bhi(v[2 * q + 1][0]) * rs + off);
    A[2][q] = pkh(blo(v[2 * q][1]) * rs + off, blo(v[2 * q + 1][1]) * rs + off);
    A[3][q] = pkh(bhi(v[2 * q][1]) * rs + off, bhi(v[2 * q + 1][1]) * rs + off);
  }
  const f4 z4 = {0.f, 0.f, 0.f, 0.f};
  LAS unsigned* zw = (LAS unsigned*)zs + w * ZSTR + r + 128 * g;
#pragma unroll
  for (int t = 0; t < 2; ++t) {
    float vr[8], vi[8];
#pragma unroll
    for (int c = 0; c < 4; ++c) {
      const f4 dc = mmh(A[c], Cm[t], z4), ds = mmh(A[c], Sm[t], z4);
#pragma unroll
      for (int b = 0; b < 2; ++b) {
        const float xr = dc[2 * b] + ds[2 * b + 1], xi = dc[2 * b + 1] - ds[2 * b];
        const int jj = 4 * b + c;
        vr[jj] = xr * twc[t][jj] + xi * tws[t][jj];
        vi[jj] = xi * twc[t][jj] - xr * tws[t][jj];
      }
    }
    u4 Vr, Vi;
#pragma unroll
    for (int q = 0; q < 4; ++q) { Vr[q] = pkh(vr[2 * q], vr[2 * q + 1]); Vi[q] = pkh(vi[2 * q], vi[2 * q + 1]); }
#pragma unroll
    for (int u = 0; u < 2; ++u) {
      f4 zr = mmh(Cm[u], Vr, z4); zr = mmh(Sm[u], Vi, zr);
      f4 zi = mmh(Cm[u], Vi, z4); zi = mmh(Nm[u], Vr, zi);
#pragma unroll
      for (int i = 0; i < 4; ++i) {
        const unsigned d = pkh(zr[i], zi[i]);
        zw[16 * t + 513 * u + 32 * i] = d;
        if (t == 0 && u == 0 && i == 0 && l == 0) zw[1025] = d;
      }
    }
  }
  __syncthreads();
  const int pos = pos_of(sp);
  const LAS unsigned* zg = (const LAS unsigned*)zs + g * ZSTR;
#pragma unroll
  for (int T = 0; T < 2; ++T) {
    const int k0 = 128 * w + 64 * T + 4 * r;
    const u2 f0 = *(const LAS u2*)(zg + k0), f1 = *(const LAS u2*)(zg + k0 + 2);
    const u2 m0 = *(const LAS u2*)(zg + 1024 - k0), m1 = *(const LAS u2*)(zg + 1022 - k0);
    const u4 F0 = {f0[0], f0[1], m0[0], m0[1]}, F1 = {f1[0], f1[1], m1[0], m1[1]};
    const f4 o0 = mmh(MA, F0, z4), o1 = mmh(MB, F0, z4), o2 = mmh(MA, F1, z4), o3 = mmh(MB, F1, z4);
    const int cg = k0 >> 4;
#pragma unroll
    for (int a = 0; a < 2; ++a) {
      const u4 o = {pkh(o0[2 * a], o0[2 * a + 1]), pkh(o1[2 * a], o1[2 * a + 1]), pkh(o2[2 * a], o2[2 * a + 1]), pkh(o3[2 * a], o3[2 * a + 1])};
      *(u4*)(G1 + (size_t)((cg * 8 + 2 * g + a) * 256 + pos) * 16 + (r & 3) * 4) = o;
    }
  }
  if (w == 0) {
    const unsigned zn = zg[513];
    const f4 o = mmh(MA, u4{zn, zn, zn, zn}, z4);
    if (r == 0) *(u2*)(G1 + (size_t)(256 * 256 + pos) * 16 + 2 * g) = u2{pkh(o[0], o[1]), pkh(o[2], o[3])};
  }
}

__global__ __launch_bounds__(256, 2) void k2_cols(const unsigned* __restrict__ G1, unsigned* __restrict__ G2,
                                               const us* __restrict__ wr, const us* __restrict__ wi) {
  __shared__ __attribute__((aligned(16))) u4 ex[2][1024];
  const int b = blockIdx.x, tid = threadIdx.x, l = tid & 63, w = __builtin_amdgcn_readfirstlane(tid >> 6);
  const int c = l & 15, g = l >> 4;
  const bool ny = (b == 256);
  const int lsrc = ny ? (l & ~8) : l;
  const unsigned* src = G1 + (size_t)b * 4096 + w * 1024 + lsrc;
  u4 B[4];
#pragma unroll
  for (int e = 0; e < 4; ++e)
#pragma unroll
    for (int i = 0; i < 4; ++i) B[e][i] = src[(e * 4 + i) * 64];
  const int rho = ny ? (c & 7) : (b & 7), col = ny ? 512 : (b >> 3) * 16 + c;
  const int woff = (8 * (4 * w + 64 * g) + rho) * DF + col;
  us wrv[4][4], wiv[4][4];
#pragma unroll
  for (int i1 = 0; i1 < 4; ++i1)
#pragma unroll
    for (int i2 = 0; i2 < 4; ++i2) {
      wrv[i1][i2] = wr[woff + 8 * (i1 + 16 * i2) * DF];
      wiv[i1][i2] = wi[woff + 8 * (i1 + 16 * i2) * DF];
    }
  u4 M1r[4], M1i[4], M2r[4], M2i[4], Fr, Fi, Ir, Ii;
#pragma unroll
  for (int e = 0; e < 4; ++e)
#pragma unroll
    for (int i = 0; i < 4; ++i) {
      const int n2 = 4 * w + e;
      const float a = (float)(((16 * (4 * g + i) + n2) * c) & 255) * (1.0f / 256.0f);
      const float ca = cosr(a) * 0.25f, sa = sinr(a) * 0.25f;
      M1r[e][i] = pkh(ca, sa); M1i[e][i] = pkh(-sa, ca);
      const float d = (float)(((16 * c + n2) * (4 * g + i)) & 255) * (1.0f / 256.0f);
      const float cd = cosr(d) * 0.25f, sd = sinr(d) * 0.25f;
      M2r[e][i] = pkh(cd, -sd); M2i[e][i] = pkh(sd, cd);
    }
#pragma unroll
  for (int i = 0; i < 4; ++i) {
    const float a = (float)((c * (4 * g + i)) & 15) * (1.0f / 16.0f);
    const float ca = cosr(a) * 0.25f, sa = sinr(a) * 0.25f;
    Fr[i] = pkh(ca, sa); Fi[i] = pkh(-sa, ca); Ir[i] = pkh(ca, -sa); Ii[i] = pkh(sa, ca);
  }
  const f4 z4 = {0.f, 0.f, 0.f, 0.f};
  {
    u4 T[4];
#pragma unroll
    for (int e = 0; e < 4; ++e) {
      const f4 re = mmh(M1r[e], B[e], z4), im = mmh(M1i[e], B[e], z4);
#pragma unroll
      for (int i = 0; i < 4; ++i) T[i][e] = pkh(re[i], im[i]);
    }
#pragma unroll
    for (int i = 0; i < 4; ++i) ex[0][(g * 4 + i) * 64 + w * 16 + c] = T[i];
  }
  __syncthreads();
  {
    u4 U[4];
#pragma unroll
    for (int i1 = 0; i1 < 4; ++i1) {
      const u4 X = ex[0][(w * 4 + i1) * 64 + l];
      const f4 re = mmh(Fr, X, z4), im = mmh(Fi, X, z4);
      u4 Y;
#pragma unroll
      for (int i2 = 0; i2 < 4; ++i2) {
        const float a = blo(wrv[i1][i2]), bb = blo(wiv[i1][i2]);
        Y[i2] = pkh(re[i2] * a - im[i2] * bb, re[i2] * bb + im[i2] * a);
      }
      const f4 r2 = mmh(Ir, Y, z4), i2v = mmh(Ii, Y, z4);
#pragma unroll
      for (int e = 0; e < 4; ++e) U[e][i1] = pkh(r2[e], i2v[e]);
    }
#pragma unroll
    for (int e = 0; e < 4; ++e) ex[1][(g * 4 + e) * 64 + w * 16 + c] = U[e];
  }
  __syncthreads();
  unsigned* dst = G2 + (size_t)b * 4096 + w * 1024 + l;
#pragma unroll
  for (int e = 0; e < 4; ++e) {
    const u4 X = ex[1][(w * 4 + e) * 64 + l];
    const f4 re = mmh(M2r[e], X, z4), im = mmh(M2i[e], X, z4);
#pragma unroll
    for (int i = 0; i < 4; ++i) dst[(e * 4 + i) * 64] = pkh(re[i], im[i]);
  }
}

DEV int zaddr(int k) {
  const int k1 = k & 31, k2 = k >> 5;
  return k1 * 32 + ((((k2 >> 2) ^ (k1 >> 2) ^ ((k1 & 2) << 1)) & 7) << 2) + (k2 & 3);
}
constexpr int YSTR = 1028;
__global__ __launch_bounds__(256, 2) void k3_rows_inv(const unsigned* __restrict__ G2, const us* __restrict__ x, const us* __restrict__ wrt,
                                                   us* __restrict__ out, us* __restrict__ ln2b, us* __restrict__ hb, unsigned* __restrict__ keys) {
  __shared__ __attribute__((aligned(16))) unsigned zs[4 * YSTR];
  const int sp = blockIdx.x, tid = threadIdx.x, l = tid & 63, w = __builtin_amdgcn_readfirstlane(tid >> 6);
  const int r = l & 15, g = l >> 4;
  const int pos = pos_of(sp);
  u4 L[2][2];
#pragma unroll
  for (int T = 0; T < 2; ++T)
#pragma unroll
    for (int a = 0; a < 2; ++a) {
      const int cg = 8 * w + 4 * T + (r >> 2);
      L[T][a] = *(const u4*)(G2 + (size_t)((cg * 8 + 2 * g + a) * 256 + pos) * 16 + (r & 3) * 4);
    }
  const u2 Ln = *(const u2*)(G2 + (size_t)(256 * 256 + pos) * 16 + 2 * g);
  const int mr = 8 * (r >> 2) + (r & 3);
  const int d0 = 32 * mr + 8 * g;
  u4 xv[2][2], wv[2];
#pragma unroll
  for (int s = 0; s < 2; ++s)
#pragma unroll
    for (int y = 0; y < 2; ++y) xv[s][y] = *(const u4*)(x + (size_t)(sp + 256 * (w + 4 * s)) * D_ + d0 + 128 * y);
#pragma unroll
  for (int y = 0; y < 2; ++y) wv[y] = *(const u4*)(wrt + d0 + 128 * y);

  u4 MA, MB;
  {
    const int ww = r >> 2, i = r & 3;
    unsigned cf[2];
#pragma unroll
    for (int a = 0; a < 2; ++a) {
      const int rho = 2 * g + a;
      const int e = (a ? -256 : 256) * ((i & 2) ? -1 : 1);
      const float an = (float)(((sp + 256 * ww) * rho + e) & 2047) * (1.0f / 2048.0f);
      const float sc = 0.70710678118f * 0.5f;
      const float cr = cosr(an) * sc, ci = -sinr(an) * sc;
      cf[a] = (i & 1) ? ((i & 2) ? pkh(ci, -cr) : pkh(-ci, cr)) : pkh(cr, ci);
    }
    MA = u4{cf[0], 0u, cf[1], 0u};
    MB = u4{0u, cf[0], 0u, cf[1]};
  }
  u4 Br[2][2], Bi[2][2], Cm[2], Sm[2], Nm[2];
  float twc[2][8], tws[2][8];
#pragma unroll
  for (int v = 0; v < 2; ++v) {
    float c[8], s[8];
#pragma unroll
    for (int j = 0; j < 8; ++j) {
      const int p = (mr + 4 * v) * (16 * (j >> 2) + 4 * g + (j & 3));
      const float a = (float)(p & 31) * (1.0f / 32.0f);
      c[j] = cosr(a); s[j] = sinr(a);
      const float bb = (float)p * (1.0f / 1024.0f);
      twc[v][j] = cosr(bb) * 0.0625f; tws[v][j] = sinr(bb) * 0.0625f;
      Br[v][j >> 2][j & 3] = pkh(c[j], -s[j]);
      Bi[v][j >> 2][j & 3] = pkh(s[j], c[j]);
    }
#pragma unroll
    for (int q = 0; q < 4; ++q) {
      Cm[v][q] = pkh(c[2 * q], c[2 * q + 1]);
      Sm[v][q] = pkh(s[2 * q], s[2 * q + 1]);
      Nm[v][q] = Sm[v][q] ^ 0x80008000u;
    }
  }
  const f4 z4 = {0.f, 0.f, 0.f, 0.f};
  LAS unsigned* zg = (LAS unsigned*)zs + g * YSTR;
#pragma unroll
  for (int T = 0; T < 2; ++T) {
    const int k0 = 128 * w + 64 * T + 4 * r;
    const u4 F0 = {L[T][0][0], L[T][0][1], L[T][1][0], L[T][1][1]}, F1 = {L[T][0][2], L[T][0][3], L[T][1][2], L[T][1][3]};
    f4 o[4] = {mmh(MA, F0, z4), mmh(MB, F0, z4), mmh(MA, F1, z4), mmh(MB, F1, z4)};
    if (T == 0) {
      const bool dc = (k0 == 0);
      o[0][0] = dc ? 0.5f * (o[0][0] + o[0][2]) : o[0][0];
      o[0][1] = dc ? 0.5f * (o[0][1] + o[0][3]) : o[0][1];
    }
#pragma unroll
    for (int xx = 0; xx < 4; ++xx) {
      const int k = k0 + xx;
      zg[zaddr(k)] = pkh(o[xx][0], o[xx][1]);
      zg[(T == 0 && xx == 0 && k == 0) ? 1024 : zaddr(1024 - k)] = pkh(o[xx][2], o[xx][3]);
    }
  }
  if (w == 0) {
    const f4 o = mmh(MA, u4{Ln[0], Ln[0], Ln[1], Ln[1]}, z4);
    if (r == 0) zg[zaddr(512)] = pkh(0.5f * (o[0] + o[2]), 0.5f * (o[1] + o[3]));
  }
  __syncthreads();
  u4 Az[2][2];
#pragma unroll
  for (int t = 0; t < 2; ++t)
#pragma unroll
    for (int u = 0; u < 2; ++u) Az[t][u] = *(const LAS u4*)((LAS unsigned*)zs + w * YSTR + zaddr(16 * t + r + 32 * (16 * u + 4 * g)));
  float yp[2][8], yq[2][8];
#pragma unroll
  for (int v = 0; v < 2; ++v) {
    float tr[8], ti[8];
#pragma unroll
    for (int t = 0; t < 2; ++t) {
      f4 re = mmh(Az[t][0], Br[v][0], z4); re = mmh(Az[t][1], Br[v][1], re);
      f4 im = mmh(Az[t][0], Bi[v][0], z4); im = mmh(Az[t][1], Bi[v][1], im);
#pragma unroll
      for (int i = 0; i < 4; ++i) {
        const int j = 4 * t + i;
        tr[j] = re[i] * twc[v][j] - im[i] * tws[v][j];
        ti[j] = re[i] * tws[v][j] + im[i] * twc[v][j];
      }
    }
    u4 Tr, Ti;
#pragma unroll
    for (int q = 0; q < 4; ++q) { Tr[q] = pkh(tr[2 * q], tr[2 * q + 1]); Ti[q] = pkh(ti[2 * q], ti[2 * q + 1]); }
#pragma unroll
    for (int y = 0; y < 2; ++y) {
      f4 re = mmh(Tr, Cm[y], z4); re = mmh(Ti, Nm[y], re);
      f4 im = mmh(Tr, Sm[y], z4); im = mmh(Ti, Cm[y], im);
#pragma unroll
      for (int i = 0; i < 4; ++i) { yp[y][4 * v + i] = re[i]; yq[y][4 * v + i] = im[i]; }
    }
  }
  u4 hv[2][2];
  float s1[2] = {0.f, 0.f}, s2[2] = {0.f, 0.f};
#pragma unroll
  for (int s = 0; s < 2; ++s)
#pragma unroll
    for (int y = 0; y < 2; ++y)
#pragma unroll
      for (int q = 0; q < 4; ++q) {
        const float y0 = s ? yq[y][2 * q] : yp[y][2 * q], y1 = s ? yq[y][2 * q + 1] : yp[y][2 * q + 1];
        const unsigned h = pkb(blo(xv[s][y][q]) + y0, bhi(xv[s][y][q]) + y1);
        hv[s][y][q] = h;
        s1[s] = dot2b(h, 0x3f803f80u, s1[s]);
        s2[s] = dot2b(h, h, s2[s]);
      }
#pragma unroll
  for (int s = 0; s < 2; ++s) {
    const float mean = sum_wave(s1[s]) * (1.0f / 1024.0f);
    float var = sum_wave(s2[s]) * (1.0f / 1024.0f) - mean * mean;
    if (__builtin_amdgcn_ballot_w64(mean * mean > 16.0f * var)) {
      float qq = 0.f;
#pragma unroll
      for (int y = 0; y < 2; ++y)
#pragma unroll
        for (int q = 0; q < 4; ++q) {
          float d = blo(hv[s][y][q]) - mean; qq += d * d;
          d = bhi(hv[s][y][q]) - mean; qq += d * d;
        }
      var = sum_wave(qq) * (1.0f / 1024.0f);
    }
    float mb, rs;
    ln_scalars(mean, var, mb, rs);
    const float off = -mb * rs;
    float rr = 0.f;
    const int rowi = sp + 256 * (w + 4 * s);
#pragma unroll
    for (int y = 0; y < 2; ++y) {
      u4 lv, ov;
#pragma unroll
      for (int q = 0; q < 4; ++q) {
        const float h0 = blo(hv[s][y][q]), h1 = bhi(hv[s][y][q]);
        const unsigned ln = pkb(h0 * rs + off, h1 * rs + off);
        lv[q] = ln;
        rr = dot2b(ln, wv[y][q], rr);
        ov[q] = pkb(h0 + blo(ln), h1 + bhi(ln));
      }
      const size_t o = (size_t)rowi * D_ + d0 + 128 * y;
      *(u4*)(out + o) = ov;
      *(u4*)(ln2b + o) = lv;
      *(u4*)(hb + o) = hv[s][y];
    }
    rr = sum_wave(rr);
    if (l == 0) {
      const unsigned rb = pkb(rr, rr) & 0xffffu;
      const unsigned mono = (rb & 0x8000u) ? (~rb & 0xffffu) : (rb | 0x8000u);
      keys[rowi] = (mono << 16) | (unsigned)(2047 - rowi);
    }
  }
}

DEV unsigned umax_wave(unsigned x) {
#define UM(CTRL) x = max(x, (unsigned)__builtin_amdgcn_update_dpp(0, (int)x, CTRL, 0xf, 0xf, true))
  UM(0xB1); UM(0x4E); UM(0x124); UM(0x128);
#undef UM
  auto a = __builtin_amdgcn_permlane16_swap(x, x, false, false);
  x = max(a[0], a[1]);
  auto b = __builtin_amdgcn_permlane32_swap(x, x, false, false);
  return max(b[0], b[1]);
}
DEV u2 trread(const LAS void* p) {
  return __builtin_bit_cast(u2, __builtin_amdgcn_ds_read_tr16_b64_v4i16((LAS s4*)p));
}
__global__ __launch_bounds__(256, 2) void k4_gateup(const unsigned* __restrict__ keys, const us* __restrict__ ln2b, const us* __restrict__ wg,
                                                 const us* __restrict__ wu, us* __restrict__ act, unsigned* __restrict__ sel) {
  __shared__ __attribute__((aligned(16))) unsigned char sm[65536 + 4096];
  const int b = blockIdx.x, tid = threadIdx.x, l = tid & 63, w = __builtin_amdgcn_readfirstlane(tid >> 6);
  const int fg = (b & 7) * 28 + (b >> 3);
  u4 kv[8];
#pragma unroll
  for (int j = 0; j < 8; ++j) kv[j] = *(const u4*)(keys + j * 256 + l * 4);
  {
    const int p = l >> 1;
    const int rowl = 256 * w + 8 * ((p >> 2) & 3) + 4 * (p >> 4) + (p & 3);
    const size_t go = (size_t)rowl * F_ + fg * 16 + (l & 1) * 8;
#pragma unroll
    for (int ks = 0; ks < 8; ++ks) {
      __builtin_amdgcn_global_load_lds((const GAS void*)(wg + go + (size_t)ks * 32 * F_), (LAS void*)(sm + w * 16384 + ks * 2048), 16, 0, 0);
      __builtin_amdgcn_global_load_lds((const GAS void*)(wu + go + (size_t)ks * 32 * F_), (LAS void*)(sm + w * 16384 + ks * 2048 + 1024), 16, 0, 0);
    }
  }
  unsigned prev = 0xffffffffu, mine = 0;
  for (int t = 0; t < 8; ++t) {
    unsigned m = 0;
#pragma unroll
    for (int j = 0; j < 8; ++j)
#pragma unroll
      for (int q = 0; q < 4; ++q) m = max(m, kv[j][q] < prev ? kv[j][q] : 0u);
    prev = umax_wave(m);
    if ((l & 7) == t) mine = prev;
  }
  if (b == 0 && tid < 8) sel[tid] = mine;
  const int rowi = 2047 - (mine & 2047);
  const int g = l >> 4;
  u4 xa[8];
#pragma unroll
  for (int ks = 0; ks < 8; ++ks) xa[ks] = *(const u4*)(ln2b + (size_t)rowi * D_ + 256 * w + 32 * ks + 8 * g);
  asm volatile("s_waitcnt vmcnt(0)" ::: "memory");
  f4 ag = {0.f, 0.f, 0.f, 0.f}, au = ag;
#pragma unroll
  for (int ks = 0; ks < 8; ++ks) {
    const LAS unsigned char* p = (const LAS unsigned char*)sm + w * 16384 + ks * 2048 + l * 8;
    const u2 g0 = trread(p), g1 = trread(p + 512), u0 = trread(p + 1024), u1 = trread(p + 1536);
    ag = mmb(xa[ks], u4{g0[0], g0[1], g1[0], g1[1]}, ag);
    au = mmb(xa[ks], u4{u0[0], u0[1], u1[0], u1[1]}, au);
  }
  LAS f4* red = (LAS f4*)(sm + 65536);
  if (l < 32) { red[w * 64 + l] = ag; red[w * 64 + 32 + l] = au; }
  __syncthreads();
  if (tid < 128) {
    const int cc = tid & 15, tok = tid >> 4;
    const LAS float* rf = (const LAS float*)red + ((tok >> 2) * 16 + cc) * 4 + (tok & 3);
    const float gs = rbf(rf[0] + rf[256] + rf[512] + rf[768]);
    const float us_ = rbf(rf[128] + rf[384] + rf[640] + rf[896]);
    const float sg = rbf(gs / (1.0f + __expf(-gs)));
    act[tok * F_ + fg * 16 + cc] = (us)(pkb(sg * us_, 0.f) & 0xffffu);
  }
}

__global__ __launch_bounds__(256, 2) void k5_down(const us* __restrict__ act, const us* __restrict__ wd, float* __restrict__ part) {
  __shared__ __attribute__((aligned(16))) unsigned char sm[4 * 7168];
  const int b = blockIdx.x, tid = threadIdx.x, l = tid & 63, w = __builtin_amdgcn_readfirstlane(tid >> 6);
  const int ki = b >> 4, nj = b & 15, g = l >> 4;
  {
    const int p = l >> 1;
    const int rowl = 224 * ki + 8 * ((p >> 2) & 3) + 4 * (p >> 4) + (p & 3);
    const size_t go = (size_t)rowl * D_ + nj * 64 + w * 16 + (l & 1) * 8;
#pragma unroll
    for (int ks = 0; ks < 7; ++ks)
      __builtin_amdgcn_global_load_lds((const GAS void*)(wd + go + (size_t)ks * 32 * D_), (LAS void*)(sm + w * 7168 + ks * 1024), 16, 0, 0);
  }
  u4 xa[7];
#pragma unroll
  for (int ks = 0; ks < 7; ++ks) xa[ks] = *(const u4*)(act + (l & 7) * F_ + 224 * ki + 32 * ks + 8 * g);
  asm volatile("s_waitcnt vmcnt(0)" ::: "memory");
  f4 acc = {0.f, 0.f, 0.f, 0.f};
#pragma unroll
  for (int ks = 0; ks < 7; ++ks) {
    const LAS unsigned char* p = (const LAS unsigned char*)sm + w * 7168 + ks * 1024 + l * 8;
    const u2 g0 = trread(p), g1 = trread(p + 512);
    acc = mmb(xa[ks], u4{g0[0], g0[1], g1[0], g1[1]}, acc);
  }
  if (l < 32) {
#pragma unroll
    for (int i = 0; i < 4; ++i) part[(size_t)(ki * 8 + 4 * g + i) * D_ + nj * 64 + w * 16 + (l & 15)] = acc[i];
  }
}

__global__ __launch_bounds__(256, 2) void k6_final(const float* __restrict__ part, const unsigned* __restrict__ sel, const us* __restrict__ ln2b,
                                                const us* __restrict__ hb, us* __restrict__ out) {
  const int idx = blockIdx.x * 256 + threadIdx.x, tok = idx >> 10, d = idx & 1023;
  const unsigned key = sel[tok];
  float y = 0.f;
#pragma unroll
  for (int i = 0; i < 16; ++i) y += part[(size_t)(i * 8 + tok) * D_ + d];
  const int rowi = 2047 - (key & 2047);
  const unsigned mono = key >> 16;
  const float rv = blo((mono & 0x8000u) ? (mono & 0x7fffu) : (~mono & 0xffffu));
  const float rs = rbf(1.0f / (1.0f + __expf(-rv)));
  const float upd = rbf(rbf(y) * rs);
  const size_t o = (size_t)rowi * D_ + d;
  const float bo = rbf(blo(ln2b[o]) + upd);
  out[o] = (us)(pkb(blo(hb[o]) + bo, 0.f) & 0xffffu);
}

extern "C" void kernel_launch(void* const* d_in, const int* in_sizes, int n_in, void* d_out, int out_size, void* d_ws, size_t ws_size,
                              hipStream_t stream) {
  const us* x = (const us*)d_in[0];
  const us* wr = (const us*)d_in[1];
  const us* wi = (const us*)d_in[2];
  const us* wrt = (const us*)d_in[4];
  const us* wg = (const us*)d_in[5];
  const us* wu = (const us*)d_in[6];
  const us* wd = (const us*)d_in[7];
  char* ws = (char*)d_ws;
  us* out = (us*)d_out;
  k1_rows_fwd<<<256, 256, 0, stream>>>(x, (unsigned*)(ws + WS_G1));
  k2_cols<<<257, 256, 0, stream>>>((const unsigned*)(ws + WS_G1), (unsigned*)(ws + WS_G2), wr, wi);
  k3_rows_inv<<<256, 256, 0, stream>>>((const unsigned*)(ws + WS_G2), x, wrt, out, (us*)(ws + WS_LN2), (us*)(ws + WS_H), (unsigned*)(ws + WS_KEY));
  k4_gateup<<<224, 256, 0, stream>>>((const unsigned*)(ws + WS_KEY), (const us*)(ws + WS_LN2), wg, wu, (us*)(ws + WS_ACT), (unsigned*)(ws + WS_SEL));
  k5_down<<<256, 256, 0, stream>>>((const us*)(ws + WS_ACT), wd, (float*)(ws + WS_PART));
  k6_final<<<32, 256, 0, stream>>>((const float*)(ws + WS_PART), (const unsigned*)(ws + WS_SEL), (const us*)(ws + WS_LN2), (const us*)(ws + WS_H), out);
}
```

```cpp
#include <hip/hip_runtime.h>
#include <stdint.h>

#define GAS __attribute__((address_space(1)))
#define LAS __attribute__((address_space(3)))
typedef _Float16 h2 __attribute__((ext_vector_type(2)));
typedef _Float16 h8 __attribute__((ext_vector_type(8)));
typedef __bf16 b2 __attribute__((ext_vector_type(2)));
typedef __bf16 b8 __attribute__((ext_vector_type(8)));
typedef float f2 __attribute__((ext_vector_type(2)));
typedef float f4 __attribute__((ext_vector_type(4)));
typedef unsigned u2 __attribute__((ext_vector_type(2)));
typedef unsigned u4 __attribute__((ext_vector_type(4)));
typedef short s4 __attribute__((ext_vector_type(4)));
typedef unsigned short us;
typedef __amdgpu_buffer_rsrc_t rsrc_t;

#define DEV __device__ __forceinline__
#define SB() __builtin_amdgcn_sched_barrier(0)

#define STAMP(K, I) do {} while (0)
#define COUNT(K, I, V) do {} while (0)
#define DUMP() do {} while (0)

#define C1 140
#define D0 40
#define D2 75
#define D3 40
#define D4 115
#define D5 35
#define D6 35
#define WS_BASE 0
#define AUX_IN 2
#define NFLY 24
#define DB 15
#define CODE_LEN 25600
#define LOADER_GATE 0u
#define AUX_OUT 16

constexpr int D_ = 1024, DF = 513, F_ = 3584;
constexpr int SC1 = 16;
constexpr unsigned SENT = 0xffffffffu;
constexpr int SPIN_CAP = 1 << 17;

constexpr int O_E = 0, O_FLAG = 2048, O_FLAGB = 4096, O_GN1 = 8192, O_S = 16384;
constexpr int O_KEY = O_S, O_XS = O_KEY + 8192, O_XH = O_XS + 16384, O_XK = O_XH + 16384, O_ACT = O_XK + 4096;
constexpr int O_PART = O_S + 131072, O_BLK = O_PART + 524288, BLK = 32896, WS_BYTES = O_BLK + 256 * BLK;
static_assert(O_ACT + 57344 <= O_PART, "small hand-off areas");
constexpr int L_GU = 0, L_DN = 65536, L_Z = L_DN + 28672, L_EX = L_Z + 16448, L_CT = L_EX + 32768, L_BYTES = L_CT + 64
;
constexpr int ZSTR = 1026, YSTR = 1028;

DEV float asf(unsigned u) { return __builtin_bit_cast(float, u); }
DEV unsigned asu(float f) { return __builtin_bit_cast(unsigned, f); }
DEV unsigned pkh(float a, float b) { h2 v = {(_Float16)a, (_Float16)b}; return __builtin_bit_cast(unsigned, v); }
DEV unsigned pkb(float a, float b) { b2 v = {(__bf16)a, (__bf16)b}; return __builtin_bit_cast(unsigned, v); }
DEV float blo(unsigned u) { return asf(u << 16); }
DEV float bhi(unsigned u) { return asf(u & 0xffff0000u); }
DEV float rbf(float a) { return blo(pkb(a, a)); }
DEV f4 mmh(u4 a, u4 b, f4 c) { return __builtin_amdgcn_mfma_f32_16x16x32_f16(__builtin_bit_cast(h8, a), __builtin_bit_cast(h8, b), c, 0, 0, 0); }
DEV f4 mmb(u4 a, u4 b, f4 c) { return __builtin_amdgcn_mfma_f32_16x16x32_bf16(__builtin_bit_cast(b8, a), __builtin_bit_cast(b8, b), c, 0, 0, 0); }
DEV float cosr(float rev) { return __builtin_amdgcn_cosf(rev); }
DEV float sinr(float rev) { return __builtin_amdgcn_sinf(rev); }
DEV float dot2b(unsigned a, unsigned b, float c) { return __builtin_amdgcn_fdot2_f32_bf16(__builtin_bit_cast(b2, a), __builtin_bit_cast(b2, b), c, false); }
DEV f2 fma2(f2 a, f2 b, f2 c) { return __builtin_elementwise_fma(a, b, c); }
template <int CTRL> DEV float dppf(float x) { return asf((unsigned)__builtin_amdgcn_update_dpp(0, (int)asu(x), CTRL, 0xf, 0xf, true)); }
DEV float swap32_add(float a, float b) { auto s = __builtin_amdgcn_permlane32_swap(asu(a), asu(b), false, false); return asf(s[0]) + asf(s[1]); }
DEV float swap16_add(float a, float b) { auto s = __builtin_amdgcn_permlane16_swap(asu(a), asu(b), false, false); return asf(s[0]) + asf(s[1]); }
DEV float row_sum(float x) { x += dppf<0xB1>(x); x += dppf<0x4E>(x); x += dppf<0x124>(x); x += dppf<0x128>(x); return x; }
DEV float sum_wave(float x) { x = row_sum(x); x = swap16_add(x, x); return swap32_add(x, x); }
DEV unsigned now32() { return (unsigned)__builtin_amdgcn_s_memrealtime(); }
DEV void wait_until(unsigned t) { while ((int)(now32() - t) < 0) __builtin_amdgcn_s_sleep(1); }
DEV void bar() { asm volatile("s_waitcnt lgkmcnt(0)\n\ts_barrier" ::: "memory"); }
DEV void pin(u4& v) { asm volatile("" : "+v"(v)); }
DEV void pin(f2& v) { asm volatile("" : "+v"(v)); }
DEV void pin(f4& v) { asm volatile("" : "+v"(v)); }
DEV void pin(float& v) { asm volatile("" : "+v"(v)); }
DEV float hadd(f2 v) { float a = v.x; pin(a); return a + v.y; }
DEV unsigned umax3(unsigned a, unsigned b, unsigned c) { return max(max(a, b), c); }
DEV unsigned umax4(u4 v) { return max(umax3(v[0], v[1], v[2]), v[3]); }
DEV u2 trread(const LAS void* p) { return __builtin_bit_cast(u2, __builtin_amdgcn_ds_read_tr16_b64_v4i16((LAS s4*)p)); }
DEV rsrc_t mkrs(const void* p, int bytes) { return __builtin_amdgcn_make_buffer_rsrc((void*)p, 0, bytes, 0x00020000); }
#define MEMB() asm volatile("" ::: "memory")

DEV u4 cmul4(f4 re, f4 im, f2 a0, f2 b0, f2 a1, f2 b1) {
  const f2 yr0 = fma2(-im.xy, b0, re.xy * a0), yi0 = fma2(im.xy, a0, re.xy * b0);
  const f2 yr1 = fma2(-im.zw, b1, re.zw * a1), yi1 = fma2(im.zw, a1, re.zw * b1);
  return u4{pkh(yr0.x, yi0.x), pkh(yr0.y, yi0.y), pkh(yr1.x, yi1.x), pkh(yr1.y, yi1.y)};
}
DEV void ln_scalars(float mean, float var, float& mb, float& rs) {
  mb = rbf(mean);
  const float sd = rbf(__builtin_amdgcn_sqrtf(rbf(rbf(var) + 1e-5f)));
  rs = __builtin_amdgcn_rcpf(sd);
}
DEV int zaddr(int k) {
  const int k1 = k & 31, k2 = k >> 5;
  return k1 * 32 + ((((k2 >> 2) ^ (k1 >> 2) ^ ((k1 & 2) << 1)) & 7) << 2) + (k2 & 3);
}

__global__ __launch_bounds__(384, 2) void fused_kernel(const us* __restrict__ x, const us* __restrict__ wr, const us* __restrict__ wi,
                                                       const us* __restrict__ wrt, const us* __restrict__ wg, const us* __restrict__ wu,
                                                       const us* __restrict__ wd, us* out, unsigned char* ws) {
  __shared__ __attribute__((aligned(16))) unsigned char sm[L_BYTES];
  unsigned long long pc0;
  asm volatile("s_getpc_b64 %0" : "=s"(pc0));
  const int b = blockIdx.x, tid = threadIdx.x, l = tid & 63, w = __builtin_amdgcn_readfirstlane(tid >> 6);
  const int r = l & 15, g = l >> 4;
  LAS unsigned char* L = (LAS unsigned char*)sm;
  volatile LAS unsigned* ct = (volatile LAS unsigned*)(L + L_CT);
  const rsrc_t rw = mkrs(ws, WS_BYTES);
  const f4 z4 = {0.f, 0.f, 0.f, 0.f};
  const int ki = (b & 7) * 2 + ((b >> 3) & 1), nj = b >> 4;
  const int fg = (b & 7) * 28 + (b >> 3);

  if (w == 4) {
    const rsrc_t rg = mkrs(wg, D_ * F_ * 2), ru = mkrs(wu, D_ * F_ * 2), rd = mkrs(wd, D_ * F_ * 2);
    const int p = l >> 1;
    const int vgu = (8 * ((p >> 2) & 3) + 4 * (p >> 4) + (p & 3)) * (F_ * 2) + fg * 32 + (l & 1) * 16;
    const int sr = l >> 3;
    const int vdn = (224 * ki + 8 * (sr >> 2) + (sr & 3)) * (D_ * 2) + nj * 128 + (((l & 7) ^ (2 * (sr >> 1))) << 4);
    const int rot = (fg & 3) * 8;
    int q = b < 224 ? 0 : 32;
    STAMP(4, 0);
    bar();
    {
      const rsrc_t rc = mkrs((const void*)(pc0 & ~255ull), CODE_LEN);
      unsigned acc = 0;
#pragma unroll
      for (int i = 0; i < (CODE_LEN + 8191) / 8192; ++i) acc |= __builtin_amdgcn_raw_buffer_load_b32(rc, l * 128, i * 8192, 0);
      _Pragma("nounroll") for (int sp = 0; sp < SPIN_CAP && ct[0] < LOADER_GATE; ++sp) __builtin_amdgcn_s_sleep(1);
      asm volatile("" ::"v"(acc));
      const rsrc_t rr = mkrs(wr, 2048 * DF * 2), ri = mkrs(wi, 2048 * DF * 2);
      const int vw = (8 * p + (b & 7)) * (DF * 2) + (b >> 3) * 32 + (l & 1) * 16;
#pragma unroll
      for (int i = 0; i < 8; ++i) {
        const int ii = (i + 2 * ((b >> 3) & 3)) & 7;
        __builtin_amdgcn_raw_ptr_buffer_load_lds(rr, (LAS void*)(L + L_EX + 16384 + ii * 1024), 16, vw, ii * 256 * DF * 2, 0, AUX_IN);
        __builtin_amdgcn_raw_ptr_buffer_load_lds(ri, (LAS void*)(L + L_EX + 24576 + ii * 1024), 16, vw, ii * 256 * DF * 2, 0, AUX_IN);
      }
    }
    const int q0 = q;
    _Pragma("nounroll") for (int ph = 1; ph <= 4; ++ph) {
      _Pragma("nounroll") while (q < 46) {
        if (ph < 4) {
          unsigned cur;
          asm volatile("ds_read_b32 %0, %1\n\ts_waitcnt lgkmcnt(0)" : "=v"(cur) : "v"((unsigned)(L_CT + 4)) : "memory");
          if (__builtin_amdgcn_readfirstlane(cur) >= ph) break;
        }
        asm volatile("s_waitcnt vmcnt(%0)" ::"n"(NFLY) : "memory");
        if (q < 32) {
          const int qq = (q + rot) & 31;
          __builtin_amdgcn_raw_ptr_buffer_load_lds(rg, (LAS void*)(L + L_GU + qq * 2048), 16, vgu, qq * 32 * F_ * 2, 0, AUX_IN);
          __builtin_amdgcn_raw_ptr_buffer_load_lds(ru, (LAS void*)(L + L_GU + qq * 2048 + 1024), 16, vgu, qq * 32 * F_ * 2, 0, AUX_IN);
        } else {
          const int j = q - 32;
          const int row = 32 * (j >> 1) + 4 * (j & 1);
          __builtin_amdgcn_raw_ptr_buffer_load_lds(rd, (LAS void*)(L + L_DN + j * 2048), 16, vdn, row * D_ * 2, 0, AUX_IN);
          __builtin_amdgcn_raw_ptr_buffer_load_lds(rd, (LAS void*)(L + L_DN + j * 2048 + 1024), 16, vdn, (row + 16) * D_ * 2, 0, AUX_IN);
        }
        ++q;
      }
      COUNT(5, ph, 1000 + q);
      if (ph == 4 || (ph == 1 && 2 * (q - q0) < NFLY)) asm volatile("s_waitcnt vmcnt(0)" ::: "memory");
      else if (ph == 1) asm volatile("s_waitcnt vmcnt(%0)" ::"n"(NFLY) : "memory");
      STAMP(4, ph);
      __builtin_amdgcn_s_barrier();
      if (ph == 2) __builtin_amdgcn_s_barrier();
    }
    DUMP();
    return;
  }

  if (w == 5) {
    if (l < 4) ct[l] = 0u;
    bar();
    const u2 ev = __builtin_amdgcn_raw_buffer_load_b64(rw, 0, O_E, SC1);
    const u4 ones = {SENT, SENT, SENT, SENT};
#pragma unroll 4
    for (int i = 0; i < 16; ++i) __builtin_amdgcn_raw_buffer_store_b128(ones, rw, l * 16, O_BLK + b * BLK + i * 1024, SC1);
    if (b < 8) __builtin_amdgcn_raw_buffer_store_b128(ones, rw, l * 16, O_GN1 + b * 1024, SC1);
    const unsigned long long e1 = (((unsigned long long)ev[1] << 32) | ev[0]) + 1ull;
    const unsigned taghi = (unsigned)(e1 >> 16), taglo = (unsigned)e1 << 16;
    asm volatile("s_waitcnt vmcnt(0)" ::: "memory");
    if (l == 0) __builtin_amdgcn_raw_buffer_store_b64(u2{taglo, taghi}, rw, 0, O_FLAG + b * 8, SC1);
    STAMP(6, 0);
#pragma unroll 4
    for (int i = 16; i < 32; ++i) __builtin_amdgcn_raw_buffer_store_b128(ones, rw, l * 16, O_BLK + b * BLK + i * 1024, SC1);
    if (l < 8) __builtin_amdgcn_raw_buffer_store_b128(ones, rw, l * 16, O_BLK + b * BLK + 32768, SC1);
    __builtin_amdgcn_raw_buffer_store_b128(ones, rw, l * 16, O_S + b * 2560, SC1);
    __builtin_amdgcn_raw_buffer_store_b128(ones, rw, l * 16 + 1024, O_S + b * 2560, SC1);
    if (l < 32) __builtin_amdgcn_raw_buffer_store_b128(ones, rw, l * 16 + 2048, O_S + b * 2560, SC1);
    _Pragma("nounroll") for (int sp = 0; sp < SPIN_CAP && ct[0] < 4u; ++sp) __builtin_amdgcn_s_sleep(1);
    const unsigned t0 = now32();
    if (l == 0) __builtin_amdgcn_raw_buffer_store_b64(u2{taglo | (t0 & 0xffffu), taghi}, rw, 0, O_FLAGB + b * 8, SC1);
    STAMP(6, 1);
    us nwr[4], nwi[4];
    u4 Fr, Fi, Ir, Ii;
    f2 tc[2], ts[2];
    if (b < 8) {
      const rsrc_t rr = mkrs(wr, 2048 * DF * 2), ri = mkrs(wi, 2048 * DF * 2);
      const int vo = ((8 * (r + 64 * g) + b) * DF + 512) * 2;
#pragma unroll
      for (int i = 0; i < 4; ++i) {
        nwr[i] = __builtin_amdgcn_raw_buffer_load_b16(rr, vo, i * 128 * DF * 2, 0);
        nwi[i] = __builtin_amdgcn_raw_buffer_load_b16(ri, vo, i * 128 * DF * 2, 0);
      }
#pragma unroll
      for (int i = 0; i < 4; ++i) {
        const int p = r * (4 * g + i);
        const float a = (float)(p & 15) * (1.0f / 16.0f);
        const float ca = cosr(a) * 0.25f, sa = sinr(a) * 0.25f;
        Fr[i] = pkh(ca, sa); Fi[i] = pkh(-sa, ca); Ir[i] = pkh(ca, -sa); Ii[i] = pkh(sa, ca);
        const float d = (float)p * (1.0f / 256.0f);
        tc[i >> 1][i & 1] = cosr(d); ts[i >> 1][i & 1] = sinr(d);
      }
    }
    wait_until(t0 + D0);
    int dmax = 0, np = 0;
#define POLL_FLAGS(OFF, DONE)                                                                                                                        \
    _Pragma("nounroll") for (int sp = 0; sp < SPIN_CAP; ++sp) {                                                                                      \
      MEMB();                                                                                                                                        \
      const u4 f0 = __builtin_amdgcn_raw_buffer_load_b128(rw, l * 16, OFF, SC1), f1 = __builtin_amdgcn_raw_buffer_load_b128(rw, l * 16, OFF + 1024, SC1); \
      const unsigned bad = (f0[1] ^ taghi) | (f0[3] ^ taghi) | (f1[1] ^ taghi) | (f1[3] ^ taghi) |                                                   \
                           (((f0[0] ^ taglo) | (f0[2] ^ taglo) | (f1[0] ^ taglo) | (f1[2] ^ taglo)) >> 16);                                          \
      ++np;                                                                                                                                          \
      if (__builtin_amdgcn_ballot_w64(bad != 0u) == 0ull) { DONE; break; }                                                                           \
      wait_until((now32() + 32u) & ~31u);                                                                                                            \
    }
    POLL_FLAGS(O_FLAG, (void)0)
    asm volatile("s_waitcnt vmcnt(0)" ::: "memory");
    STAMP(6, 2);
    COUNT(5, 0, 1000 + np);
    bar();
    if (b == 0 && l == 0) __builtin_amdgcn_raw_buffer_store_b64(u2{(unsigned)e1, (unsigned)(e1 >> 32)}, rw, 0, O_E, SC1);
    wait_until(now32() + DB);
    np = 0;
    POLL_FLAGS(O_FLAGB, dmax = max(max((int)(short)(f0[0] - t0), (int)(short)(f0[2] - t0)), max((int)(short)(f1[0] - t0), (int)(short)(f1[2] - t0))))
#undef POLL_FLAGS
#define IM(CTRL) dmax = max(dmax, __builtin_amdgcn_update_dpp(0, dmax, CTRL, 0xf, 0xf, true))
    IM(0xB1); IM(0x4E); IM(0x124); IM(0x128);
#undef IM
    dmax = max(max(__builtin_amdgcn_readlane(dmax, 0), __builtin_amdgcn_readlane(dmax, 16)), max(__builtin_amdgcn_readlane(dmax, 32), __builtin_amdgcn_readlane(dmax, 48)));
    const unsigned tn = now32();
    const unsigned T1 = (((int)(t0 + dmax - tn) > 0 ? tn : t0 + dmax) + C1) | 1u;
    if (l == 0) ct[2] = T1;
    STAMP(6, 3);
    COUNT(5, 5, 1000 + np);
    DUMP();
    if (b >= 8) return;
    wait_until(T1);
    u4 U;
    _Pragma("nounroll") for (int sp = 0; sp < SPIN_CAP; ++sp) {
      MEMB();
      U = __builtin_amdgcn_raw_buffer_load_b128(rw, l * 16, O_GN1 + b * 1024, SC1);
      if (__builtin_amdgcn_ballot_w64(umax4(U) == SENT) == 0ull) break;
      __builtin_amdgcn_s_sleep(4);
    }
    f4 re = mmh(U, Fr, z4), im = mmh(U, Fi, z4);
    U = cmul4(re, im, tc[0], -ts[0], tc[1], -ts[1]);
    pin(U);
    __builtin_amdgcn_s_barrier();
    re = mmh(Fr, U, z4); im = mmh(Fi, U, z4);
    U = cmul4(re, im, f2{blo(nwr[0]), blo(nwr[1])}, f2{blo(nwi[0]), blo(nwi[1])}, f2{blo(nwr[2]), blo(nwr[3])}, f2{blo(nwi[2]), blo(nwi[3])});
    re = mmh(U, Ir, z4); im = mmh(U, Ii, z4);
    U = cmul4(re, im, tc[0], ts[0], tc[1], ts[1]);
    re = mmh(Ir, U, z4); im = mmh(Ii, U, z4);
#pragma unroll
    for (int i = 0; i < 4; ++i)
      __builtin_amdgcn_raw_buffer_store_b32(pkh(re[i], im[i]), rw, (64 * g + r) * BLK, O_BLK + 16 * i * BLK + 32768 + b * 4, SC1);
    MEMB();
    __builtin_amdgcn_s_barrier();
    return;
  }

  STAMP(0, 0);
  const rsrc_t rx = mkrs(x, 2048 * D_ * 2);
  u2 v[8];
  {
    const int sig = (r >> 1) & 1, beta = 2 * (r >> 2) + (r & 1);
    const int vo = sig * 1024 * D_ * 2 + (256 * g + 4 * beta) * 2;
#pragma unroll
    for (int j = 0; j < 8; ++j) v[j] = __builtin_amdgcn_raw_buffer_load_b64(rx, vo + 64 * j, (b + 256 * w) * D_ * 2, AUX_IN);
  }
  SB();
  bar();
  SB();
  {
    u4 Cm[2], Sm[2], Nm[2];
    f2 twc[2][4], tws[2][4];
#pragma unroll
    for (int t = 0; t < 2; ++t) {
      float c[8], s[8];
#pragma unroll
      for (int j = 0; j < 8; ++j) {
        const int p = (8 * g + j) * (16 * t + r);
        const float a = (float)(p & 31) * (1.0f / 32.0f);
        c[j] = cosr(a); s[j] = sinr(a);
        const float bb = (float)p * (1.0f / 1024.0f);
        twc[t][j & 3][j >> 2] = cosr(bb) * 0.125f; tws[t][j & 3][j >> 2] = sinr(bb) * 0.125f;
      }
#pragma unroll
      for (int q = 0; q < 4; ++q) {
        Cm[t][q] = pkh(c[2 * q], c[2 * q + 1]);
        Sm[t][q] = pkh(s[2 * q], s[2 * q + 1]);
        Nm[t][q] = Sm[t][q] ^ 0x80008000u;
        pin(twc[t][q]); pin(tws[t][q]);
      }
      pin(Cm[t]); pin(Sm[t]); pin(Nm[t]);
    }
    u4 MA, MB;
    {
      const int rho = r >> 1, part = r & 1;
      const int base = (b + 256 * g) * rho + 512 * part;
      const int e = (rho & 1) ? -256 : 256;
      const float aa = (float)((base + e) & 2047) * (1.0f / 2048.0f), ab = (float)((base - e) & 2047) * (1.0f / 2048.0f);
      const float sc = 0.70710678118f / 16.0f;
      const unsigned a0 = pkh(cosr(aa) * sc, sinr(aa) * sc), a1 = pkh(cosr(ab) * sc, -sinr(ab) * sc);
      MA = u4{a0, 0u, 0u, a1};
      MB = u4{0u, a0, a1, 0u};
      pin(MA); pin(MB);
    }
    SB();
    STAMP(0, 1);
    float s1 = 0.f, s2 = 0.f, s1b = 0.f, s2b = 0.f;
#pragma unroll
    for (int j = 0; j < 8; ++j) {
      s1 = dot2b(v[j][0], 0x3f803f80u, s1); s1b = dot2b(v[j][1], 0x3f803f80u, s1b);
      s2 = dot2b(v[j][0], v[j][0], s2); s2b = dot2b(v[j][1], v[j][1], s2b);
    }
    SB();
    if (l == 0) __hip_atomic_fetch_add((LAS unsigned*)(L + L_CT), 1u, __ATOMIC_RELAXED, __HIP_MEMORY_SCOPE_WORKGROUP);
    STAMP(0, 2);
    float st = swap32_add(s1 + s1b, s2 + s2b);
    st = swap16_add(st, st);
    st += dppf<0xB1>(st); st += dppf<0x124>(st); st += dppf<0x128>(st);
    auto sw = __builtin_amdgcn_permlane32_swap(asu(st), asu(st), false, false);
    const float mean = asf(sw[0]) * (1.0f / 1024.0f);
    float var = asf(sw[1]) * (1.0f / 1024.0f) - mean * mean;
    if (__builtin_amdgcn_ballot_w64(mean * mean > 16.0f * var)) {
      float q = 0.f;
#pragma unroll
      for (int j = 0; j < 8; ++j) {
        float d;
        d = blo(v[j][0]) - mean; q += d * d; d = bhi(v[j][0]) - mean; q += d * d;
        d = blo(v[j][1]) - mean; q += d * d; d = bhi(v[j][1]) - mean; q += d * d;
      }
      q = swap32_add(q, q); q = swap16_add(q, q);
      q += dppf<0xB1>(q); q += dppf<0x124>(q); q += dppf<0x128>(q);
      var = q * (1.0f / 1024.0f);
    }
    float mb, rs;
    ln_scalars(mean, var, mb, rs);
    const f2 rs2 = {rs, rs}, off2 = {-mb * rs, -mb * rs};
    u4 A[4];
#pragma unroll
    for (int q = 0; q < 4; ++q) {
      f2 t;
      t = fma2(f2{blo(v[2 * q][0]), blo(v[2 * q + 1][0])}, rs2, off2); A[0][q] = pkh(t.x, t.y);
      t = fma2(f2{bhi(v[2 * q][0]), bhi(v[2 * q + 1][0])}, rs2, off2); A[1][q] = pkh(t.x, t.y);
      t = fma2(f2{blo(v[2 * q][1]), blo(v[2 * q + 1][1])}, rs2, off2); A[2][q] = pkh(t.x, t.y);
      t = fma2(f2{bhi(v[2 * q][1]), bhi(v[2 * q + 1][1])}, rs2, off2); A[3][q] = pkh(t.x, t.y);
    }
    if (tid == 0) ct[1] = 1u;
    LAS unsigned* zw = (LAS unsigned*)(L + L_Z) + w * ZSTR + r + 128 * g;
#pragma unroll
    for (int t = 0; t < 2; ++t) {
      f2 vr[4], vi[4];
#pragma unroll
      for (int c = 0; c < 4; ++c) {
        const f4 dc = mmh(A[c], Cm[t], z4), ds = mmh(A[c], Sm[t], z4);
        const f2 xr = dc.xy + ds.zw, xi = dc.zw - ds.xy;
        vr[c] = fma2(xi, tws[t][c], xr * twc[t][c]);
        vi[c] = fma2(-xr, tws[t][c], xi * twc[t][c]);
      }
      const u4 Vr = {pkh(vr[0].x, vr[1].x), pkh(vr[2].x, vr[3].x), pkh(vr[0].y, vr[1].y), pkh(vr[2].y, vr[3].y)};
      const u4 Vi = {pkh(vi[0].x, vi[1].x), pkh(vi[2].x, vi[3].x), pkh(vi[0].y, vi[1].y), pkh(vi[2].y, vi[3].y)};
#pragma unroll
      for (int u = 0; u < 2; ++u) {
        f4 zr = mmh(Cm[u], Vr, z4); zr = mmh(Sm[u], Vi, zr);
        f4 zi = mmh(Cm[u], Vi, z4); zi = mmh(Nm[u], Vr, zi);
#pragma unroll
        for (int i = 0; i < 4; ++i) {
          const unsigned d = pkh(zr[i], zi[i]);
          zw[16 * t + 513 * u + 32 * i] = d;
          if (t == 0 && u == 0 && i == 0 && l == 0) zw[1025] = d;
        }
      }
    }
    STAMP(0, 3);
    bar();
    STAMP(0, 4);
    const int pos = (b & 15) * 16 + ((b >> 4) & 3) * 4 + (b >> 6);
    const LAS unsigned* zg = (const LAS unsigned*)(L + L_Z) + g * ZSTR;
    const int vo = ((r >> 2) * 8 + 2 * g) * BLK + (r & 3) * 16;
    u4 og[2][2];
#pragma unroll
    for (int T = 0; T < 2; ++T) {
      const int k0 = 128 * w + 64 * T + 4 * r;
      const u2 f0 = *(const LAS u2*)(zg + k0), f1 = *(const LAS u2*)(zg + k0 + 2);
      const u2 m0 = *(const LAS u2*)(zg + 1024 - k0), m1 = *(const LAS u2*)(zg + 1022 - k0);
      const u4 F0 = {f0[0], f0[1], m0[0], m0[1]}, F1 = {f1[0], f1[1], m1[0], m1[1]};
      const f4 o0 = mmh(MA, F0, z4), o1 = mmh(MB, F0, z4), o2 = mmh(MA, F1, z4), o3 = mmh(MB, F1, z4);
#pragma unroll
      for (int a = 0; a < 2; ++a) {
        og[T][a] = u4{pkh(o0[2 * a], o0[2 * a + 1]), pkh(o1[2 * a], o1[2 * a + 1]), pkh(o2[2 * a], o2[2 * a + 1]), pkh(o3[2 * a], o3[2 * a + 1])};
        __builtin_amdgcn_raw_buffer_store_b128(og[T][a], rw, vo, O_BLK + ((8 * w + 4 * T) * 8 + a) * BLK + pos * 64, SC1);
      }
    }
    asm volatile("s_nop 1" ::"v"(og[0][0]), "v"(og[0][1]), "v"(og[1][0]), "v"(og[1][1]));
    if (w == 3) {
      const unsigned zn = zg[513];
      const f4 o = mmh(MA, u4{zn, zn, zn, zn}, z4);
      const int pn = ((b >> 6) * 16 + (b & 15)) * 4 + ((b >> 4) & 3);
      if (r < 2) __builtin_amdgcn_raw_buffer_store_b32(r ? pkh(o[2], o[3]) : pkh(o[0], o[1]), rw, (2 * g + r) * 1024, O_GN1 + pn * 4, SC1);
    }
    STAMP(0, 5);
  }
  unsigned T2;
  {
    SB();
    u4 M1r[4], M1i[4], M2r[4], M2i[4], Fr, Fi, Ir, Ii;
#pragma unroll
    for (int e = 0; e < 4; ++e) {
#pragma unroll
      for (int i = 0; i < 4; ++i) {
        const float a = (float)(((16 * (4 * g + i) + 4 * w + e) * r) & 255) * (1.0f / 256.0f);
        const float ca = cosr(a) * 0.25f, sa = sinr(a) * 0.25f;
        M1r[e][i] = pkh(ca, sa); M1i[e][i] = pkh(-sa, ca);
      }
      pin(M1r[e]); pin(M1i[e]);
    }
#pragma unroll
    for (int i = 0; i < 4; ++i) {
      const float a = (float)((r * (4 * g + i)) & 15) * (1.0f / 16.0f);
      const float ca = cosr(a) * 0.25f, sa = sinr(a) * 0.25f;
      Fr[i] = pkh(ca, sa); Fi[i] = pkh(-sa, ca); Ir[i] = pkh(ca, -sa); Ii[i] = pkh(sa, ca);
    }
    pin(Fr); pin(Fi); pin(Ir); pin(Ii);
    f2 wa[4][2], wb[4][2];
#pragma unroll
    for (int i1 = 0; i1 < 4; ++i1) {
      const LAS unsigned char* p = L + L_EX + 16384 + (4 * w + i1 + 64 * g + 16 * (r >> 2)) * 32 + (r & 3) * 8;
      const u2 a = trread(p), c = trread(p + 8192);
#pragma unroll
      for (int h = 0; h < 2; ++h) {
        wa[i1][h] = f2{blo(a[h]), bhi(a[h])};
        wb[i1][h] = f2{blo(c[h]), bhi(c[h])};
        pin(wa[i1][h]); pin(wb[i1][h]);
      }
    }
    SB();
    STAMP(1, 0);
    unsigned T1 = 0;
    _Pragma("nounroll") for (int sp = 0; sp < 64 * SPIN_CAP && (T1 = __builtin_amdgcn_readfirstlane(ct[2])) == 0u; ++sp) __builtin_amdgcn_s_sleep(1);
    wait_until(T1);
    if (tid == 0) ct[1] = 2u;
    STAMP(1, 1);
    u4 B[4];
    const int so = O_BLK + b * BLK + w * 4096;
#pragma unroll
    for (int e = 0; e < 4; ++e)
#pragma unroll
      for (int i = 0; i < 4; ++i) B[e][i] = __builtin_amdgcn_raw_buffer_load_b32(rw, l * 4 + (e * 4 + i) * 256, so, SC1);
    SB();
#pragma unroll
    for (int e = 0; e < 4; ++e) {
#pragma unroll
      for (int i = 0; i < 4; ++i) {
        const float d = (float)(((16 * r + 4 * w + e) * (4 * g + i)) & 255) * (1.0f / 256.0f);
        const float cd = cosr(d) * 0.25f, sd = sinr(d) * 0.25f;
        M2r[e][i] = pkh(cd, -sd); M2i[e][i] = pkh(sd, cd);
      }
      pin(M2r[e]); pin(M2i[e]);
    }
    SB();
    STAMP(1, 2);
    int np = 0;
    _Pragma("nounroll") for (int sp = 0; sp < SPIN_CAP; ++sp) {
      if (__builtin_amdgcn_ballot_w64(max(max(umax4(B[0]), umax4(B[1])), max(umax4(B[2]), umax4(B[3]))) == SENT) == 0ull) break;
      MEMB();
      ++np;
#pragma unroll
      for (int e = 0; e < 4; ++e)
#pragma unroll
        for (int i = 0; i < 4; ++i) B[e][i] = __builtin_amdgcn_raw_buffer_load_b32(rw, l * 4 + (e * 4 + i) * 256, so, SC1);
    }
    STAMP(1, 3);
    COUNT(7, 0, 1000 + np);
    LAS u4* ex = (LAS u4*)(L + L_EX);
    {
      u4 T[4];
#pragma unroll
      for (int e = 0; e < 4; ++e) {
        const f4 re = mmh(M1r[e], B[e], z4), im = mmh(M1i[e], B[e], z4);
#pragma unroll
        for (int i = 0; i < 4; ++i) T[i][e] = pkh(re[i], im[i]);
      }
#pragma unroll
      for (int i = 0; i < 4; ++i) ex[(g * 4 + i) * 64 + w * 16 + r] = T[i];
    }
    bar();
    {
      u4 U[4];
#pragma unroll
      for (int i1 = 0; i1 < 4; ++i1) {
        const u4 X = ex[(w * 4 + i1) * 64 + l];
        const f4 re = mmh(Fr, X, z4), im = mmh(Fi, X, z4);
        const u4 Y = cmul4(re, im, wa[i1][0], wb[i1][0], wa[i1][1], wb[i1][1]);
        const f4 r2 = mmh(Ir, Y, z4), i2v = mmh(Ii, Y, z4);
#pragma unroll
        for (int e = 0; e < 4; ++e) U[e][i1] = pkh(r2[e], i2v[e]);
      }
#pragma unroll
      for (int e = 0; e < 4; ++e) ex[1024 + (g * 4 + e) * 64 + w * 16 + r] = U[e];
    }
    bar();
    const int vo = 64 * g * BLK + r * 4, so2 = O_BLK + 4 * w * BLK + 16384 + (b & 7) * 2048 + (b >> 3) * 64;
#pragma unroll
    for (int e = 0; e < 4; ++e) {
      const u4 X = ex[1024 + (w * 4 + e) * 64 + l];
      const f4 re = mmh(M2r[e], X, z4), im = mmh(M2i[e], X, z4);
#pragma unroll
      for (int i = 0; i < 4; ++i) __builtin_amdgcn_raw_buffer_store_b32(pkh(re[i], im[i]), rw, vo, so2 + (16 * i + e) * BLK, SC1);
    }
    SB();
    T2 = now32() + D2;
    STAMP(1, 4);
  }
  const int mr = 8 * (r >> 2) + (r & 3);
  const int d0 = 32 * mr + 8 * g;
  f4 hp[2][2], hq[2][2];
  f2 wf[2][4];
  float wsum, wthr;
  {
    u4 xv[2][2], wv[2];
    const rsrc_t rt = mkrs(wrt, D_ * 2);
#pragma unroll
    for (int s = 0; s < 2; ++s)
#pragma unroll
      for (int y = 0; y < 2; ++y) xv[s][y] = __builtin_amdgcn_raw_buffer_load_b128(rx, d0 * 2 + 256 * y, (b + 256 * (w + 4 * s)) * D_ * 2, 0);
#pragma unroll
    for (int y = 0; y < 2; ++y) wv[y] = __builtin_amdgcn_raw_buffer_load_b128(rt, d0 * 2 + 256 * y, 0, 0);
    SB();
    u4 MA, MB;
    {
      const int ww = r >> 2, i = r & 3;
      unsigned cf[2];
#pragma unroll
      for (int a = 0; a < 2; ++a) {
        const int e = ((i & 2) ? -1 : 1) * ((a ? -256 : 256) - 512 * (i & 1));
        const float an = (float)(((b + 256 * ww) * (2 * g + a) + e) & 2047) * (1.0f / 2048.0f);
        const float sc = 0.70710678118f * 0.5f;
        cf[a] = pkh(cosr(an) * sc, -sinr(an) * sc);
      }
      MA = u4{cf[0], 0u, cf[1], 0u};
      MB = u4{0u, cf[0], 0u, cf[1]};
      pin(MA); pin(MB);
    }
    u4 Br[2][2], Bi[2][2], Cm[2], Sm[2], Nm[2];
#pragma unroll
    for (int vv = 0; vv < 2; ++vv) {
      float c[8], s[8];
#pragma unroll
      for (int j = 0; j < 8; ++j) {
        const int p = (mr + 4 * vv) * (16 * (j >> 2) + 4 * g + (j & 3));
        const float a = (float)(p & 31) * (1.0f / 32.0f);
        c[j] = cosr(a); s[j] = sinr(a);
        Br[vv][j >> 2][j & 3] = pkh(c[j], -s[j]);
        Bi[vv][j >> 2][j & 3] = pkh(s[j], c[j]);
      }
#pragma unroll
      for (int q = 0; q < 4; ++q) {
        Cm[vv][q] = pkh(c[2 * q], c[2 * q + 1]);
        Sm[vv][q] = pkh(s[2 * q], s[2 * q + 1]);
        Nm[vv][q] = Sm[vv][q] ^ 0x80008000u;
      }
      pin(Br[vv][0]); pin(Br[vv][1]); pin(Bi[vv][0]); pin(Bi[vv][1]); pin(Cm[vv]); pin(Sm[vv]); pin(Nm[vv]);
    }
    SB();
    STAMP(2, 0);
    wait_until(T2);
    if (tid == 0) ct[1] = 3u;
    STAMP(2, 1);
    u4 Lq[2][2];
    u2 Ln;
    const int vo = g * 4096 + r * 16, so = O_BLK + b * BLK + 16384 + w * 512;
#pragma unroll
    for (int T = 0; T < 2; ++T)
#pragma unroll
      for (int a = 0; a < 2; ++a) Lq[T][a] = __builtin_amdgcn_raw_buffer_load_b128(rw, vo + a * 2048 + T * 256, so, SC1);
    Ln = __builtin_amdgcn_raw_buffer_load_b64(rw, g * 8, O_BLK + b * BLK + 32768, SC1);
    SB();
    f2 twc[2][4], tws[2][4];
#pragma unroll
    for (int vv = 0; vv < 2; ++vv)
#pragma unroll
      for (int j = 0; j < 8; ++j) {
        const float bb = (float)((mr + 4 * vv) * (16 * (j >> 2) + 4 * g + (j & 3))) * (1.0f / 1024.0f);
        twc[vv][j >> 1][j & 1] = cosr(bb) * 0.0625f; tws[vv][j >> 1][j & 1] = sinr(bb) * 0.0625f;
        if (j & 1) { pin(twc[vv][j >> 1]); pin(tws[vv][j >> 1]); }
      }
    float ws1 = 0.f, ws2 = 0.f;
#pragma unroll
    for (int y = 0; y < 2; ++y) {
#pragma unroll
      for (int vv = 0; vv < 2; ++vv) {
        hp[y][vv] = f4{blo(xv[0][y][2 * vv]), bhi(xv[0][y][2 * vv]), blo(xv[0][y][2 * vv + 1]), bhi(xv[0][y][2 * vv + 1])};
        hq[y][vv] = f4{blo(xv[1][y][2 * vv]), bhi(xv[1][y][2 * vv]), blo(xv[1][y][2 * vv + 1]), bhi(xv[1][y][2 * vv + 1])};
        pin(hp[y][vv]); pin(hq[y][vv]);
      }
#pragma unroll
      for (int q = 0; q < 4; ++q) {
        wf[y][q] = f2{blo(wv[y][q]), bhi(wv[y][q])};
        pin(wf[y][q]);
        ws1 = dot2b(wv[y][q], 0x3f803f80u, ws1); ws2 = dot2b(wv[y][q], wv[y][q], ws2);
      }
    }
    wsum = sum_wave(ws1);
    wthr = 2.0f * __builtin_amdgcn_sqrtf(sum_wave(ws2));
    pin(wsum); pin(wthr);
    int za[2][4], zb[2][4];
#pragma unroll
    for (int T = 0; T < 2; ++T)
#pragma unroll
      for (int xx = 0; xx < 4; ++xx) {
        const int k = 128 * w + 64 * T + 4 * r + xx;
        za[T][xx] = L_Z + (g * YSTR + zaddr(k)) * 4;
        zb[T][xx] = L_Z + (g * YSTR + (k == 0 ? 1024 : zaddr(1024 - k))) * 4;
        asm volatile("" : "+v"(za[T][xx]), "+v"(zb[T][xx]));
      }
    SB();
    STAMP(2, 2);
    int np = 0;
    _Pragma("nounroll") for (int sp = 0; sp < SPIN_CAP; ++sp) {
      if (__builtin_amdgcn_ballot_w64(max(umax3(umax4(Lq[0][0]), umax4(Lq[0][1]), Ln[0]), umax3(umax4(Lq[1][0]), umax4(Lq[1][1]), Ln[1])) == SENT) == 0ull) break;
      MEMB();
      ++np;
#pragma unroll
      for (int T = 0; T < 2; ++T)
#pragma unroll
        for (int a = 0; a < 2; ++a) Lq[T][a] = __builtin_amdgcn_raw_buffer_load_b128(rw, vo + a * 2048 + T * 256, so, SC1);
      Ln = __builtin_amdgcn_raw_buffer_load_b64(rw, g * 8, O_BLK + b * BLK + 32768, SC1);
    }
    STAMP(2, 3);
    COUNT(7, 1, 1000 + np);
#pragma unroll
    for (int T = 0; T < 2; ++T) {
      const u4 F0 = {Lq[T][0][0], Lq[T][0][1], Lq[T][1][0], Lq[T][1][1]}, F1 = {Lq[T][0][2], Lq[T][0][3], Lq[T][1][2], Lq[T][1][3]};
      f4 o[4] = {mmh(MA, F0, z4), mmh(MB, F0, z4), mmh(MA, F1, z4), mmh(MB, F1, z4)};
      if (T == 0 && w == 0) {
        o[0][0] = r == 0 ? 0.5f * (o[0][0] + o[0][2]) : o[0][0];
        o[0][1] = r == 0 ? 0.5f * (o[0][1] + o[0][3]) : o[0][1];
      }
#pragma unroll
      for (int xx = 0; xx < 4; ++xx) {
        *(LAS unsigned*)(L + za[T][xx]) = pkh(o[xx][0], o[xx][1]);
        *(LAS unsigned*)(L + zb[T][xx]) = pkh(o[xx][2], o[xx][3]);
      }
    }
    if (w == 3) {
      const f4 o = mmh(MA, u4{Ln[0], Ln[0], Ln[1], Ln[1]}, z4);
      if (r == 0) ((LAS unsigned*)(L + L_Z))[g * YSTR + zaddr(512)] = pkh(0.5f * (o[0] + o[2]), 0.5f * (o[1] + o[3]));
    }
    bar();
    u4 Az[2][2];
#pragma unroll
    for (int t = 0; t < 2; ++t)
#pragma unroll
      for (int u = 0; u < 2; ++u) Az[t][u] = *(const LAS u4*)((LAS unsigned*)(L + L_Z) + w * YSTR + zaddr(16 * t + r + 32 * (16 * u + 4 * g)));
#pragma unroll
    for (int vv = 0; vv < 2; ++vv) {
      u4 Tr, Ti;
#pragma unroll
      for (int t = 0; t < 2; ++t) {
        f4 re = mmh(Az[t][0], Br[vv][0], z4); re = mmh(Az[t][1], Br[vv][1], re);
        f4 im = mmh(Az[t][0], Bi[vv][0], z4); im = mmh(Az[t][1], Bi[vv][1], im);
        const f2 a0 = fma2(-im.xy, tws[vv][2 * t], re.xy * twc[vv][2 * t]), b0 = fma2(im.xy, twc[vv][2 * t], re.xy * tws[vv][2 * t]);
        const f2 a1 = fma2(-im.zw, tws[vv][2 * t + 1], re.zw * twc[vv][2 * t + 1]), b1 = fma2(im.zw, twc[vv][2 * t + 1], re.zw * tws[vv][2 * t + 1]);
        Tr[2 * t] = pkh(a0.x, a0.y); Tr[2 * t + 1] = pkh(a1.x, a1.y);
        Ti[2 * t] = pkh(b0.x, b0.y); Ti[2 * t + 1] = pkh(b1.x, b1.y);
      }
#pragma unroll
      for (int y = 0; y < 2; ++y) {
        hp[y][vv] = mmh(Ti, Nm[y], mmh(Tr, Cm[y], hp[y][vv]));
        hq[y][vv] = mmh(Ti, Cm[y], mmh(Tr, Sm[y], hq[y][vv]));
      }
    }
  }
  STAMP(2, 4);
  unsigned k0, k1, Tk;
  bool likely;
  u4 lv[2][2], hv[2][2], ov[2][2];
  {
    f2 a1[2] = {{0.f, 0.f}, {0.f, 0.f}}, a2[2] = {{0.f, 0.f}, {0.f, 0.f}}, a3[2] = {{0.f, 0.f}, {0.f, 0.f}};
#pragma unroll
    for (int y = 0; y < 2; ++y)
#pragma unroll
      for (int q = 0; q < 4; ++q) {
        const f2 p = (q & 1) ? hp[y][q >> 1].zw : hp[y][q >> 1].xy, qq = (q & 1) ? hq[y][q >> 1].zw : hq[y][q >> 1].xy;
        a1[0] += p; a2[0] = fma2(p, p, a2[0]); a3[0] = fma2(p, wf[y][q], a3[0]);
        a1[1] += qq; a2[1] = fma2(qq, qq, a2[1]); a3[1] = fma2(qq, wf[y][q], a3[1]);
      }
    const float t1 = swap32_add(hadd(a1[0]), hadd(a1[1])), t2 = swap32_add(hadd(a2[0]), hadd(a2[1]));
    float t3 = swap32_add(hadd(a3[0]), hadd(a3[1]));
    const float t12 = row_sum(swap16_add(t1, t2));
    t3 = row_sum(swap16_add(t3, t3));
    auto sw = __builtin_amdgcn_permlane16_swap(asu(t12), asu(t12), false, false);
    const float mean = asf(sw[0]) * (1.0f / 1024.0f);
    float var = asf(sw[1]) * (1.0f / 1024.0f) - mean * mean;
    float mref = 0.f;
    if (__builtin_amdgcn_ballot_w64(mean * mean > 16.0f * var)) {
      const float mp = asf(__builtin_amdgcn_readlane(asu(mean), 0)), mq = asf(__builtin_amdgcn_readlane(asu(mean), 32));
      float sp = 0.f, sq = 0.f, dp = 0.f, dq = 0.f;
#pragma unroll
      for (int y = 0; y < 2; ++y)
#pragma unroll
        for (int j = 0; j < 8; ++j) {
          const float e1 = hp[y][j >> 2][j & 3] - mp, e2 = hq[y][j >> 2][j & 3] - mq;
          sp += e1 * e1; sq += e2 * e2;
          dp += e1 * wf[y][j >> 1][j & 1]; dq += e2 * wf[y][j >> 1][j & 1];
        }
      float u1 = swap32_add(sp, sq), u2_ = swap32_add(dp, dq);
      var = row_sum(swap16_add(u1, u1)) * (1.0f / 1024.0f);
      t3 = row_sum(swap16_add(u2_, u2_));
      mref = mean;
    }
    float mb, rs;
    ln_scalars(mean, var, mb, rs);
    const float rr = (t3 + (mref - mb) * wsum) * rs;
    const unsigned rb = pkb(rr, rr) & 0xffffu;
    const unsigned inv = (rb & 0x8000u) ? rb : (rb ^ 0x7fffu);
    const unsigned key = (inv << 16) | (unsigned)(b + 256 * w + 32 * (l & 32));
    if ((l & 31) == 0) __builtin_amdgcn_raw_buffer_store_b32(key, rw, l >> 3, O_KEY + (b * 4 + w) * 8, SC1);
    SB();
    Tk = now32();
    STAMP(2, 5);
    k0 = __builtin_amdgcn_readlane(key, 0); k1 = __builtin_amdgcn_readlane(key, 32);
    likely = __builtin_amdgcn_ballot_w64(!(rr <= wthr)) != 0ull;
    const float off = -mb * rs;
#pragma unroll
    for (int s = 0; s < 2; ++s) {
      const float rs_ = asf(__builtin_amdgcn_readlane(asu(rs), 32 * s)), of_ = asf(__builtin_amdgcn_readlane(asu(off), 32 * s));
      const f2 r2 = {rs_, rs_}, o2 = {of_, of_};
#pragma unroll
      for (int y = 0; y < 2; ++y)
#pragma unroll
        for (int q = 0; q < 4; ++q) {
          const f4 h4 = s ? hq[y][q >> 1] : hp[y][q >> 1];
          const f2 h = (q & 1) ? h4.zw : h4.xy;
          const f2 ln = fma2(h, r2, o2), o = h + ln;
          lv[s][y][q] = pkb(ln.x, ln.y); hv[s][y][q] = pkb(h.x, h.y); ov[s][y][q] = pkb(o.x, o.y);
        }
    }
  }
  const rsrc_t ro = mkrs(out, 2048 * D_ * 2);
  {
    const unsigned kb = min(k0, k1), ks = max(k0, k1);
    bool open = true, sel0 = false, sel1 = false;
    if (!likely) {
#pragma unroll
      for (int s = 0; s < 2; ++s)
#pragma unroll
        for (int y = 0; y < 2; ++y) __builtin_amdgcn_raw_buffer_store_b128(ov[s][y], ro, d0 * 2 + 256 * y, (b + 256 * (w + 4 * s)) * D_ * 2, AUX_OUT);
      wait_until(Tk + D3);
      const u4 kq = __builtin_amdgcn_raw_buffer_load_b128(rw, l * 16, O_KEY + ((b >> 3) & 7) * 1024, SC1);
      int n = 0;
#pragma unroll
      for (int j = 0; j < 4; ++j) n += __builtin_popcountll(__builtin_amdgcn_ballot_w64(kq[j] < kb));
      open = n < 8;
    }
    if (open) {
      wait_until(Tk + D3);
      u4 kv[8];
      int nb = 8;
      _Pragma("nounroll") for (int sp = 0; sp < SPIN_CAP; ++sp) {
        MEMB();
#pragma unroll
        for (int j = 0; j < 8; ++j) kv[j] = __builtin_amdgcn_raw_buffer_load_b128(rw, l * 16 + (j & 3) * 1024, O_KEY + (j >> 2) * 4096, SC1);
        nb = 0;
        unsigned mx = 0;
#pragma unroll
        for (int j = 0; j < 8; ++j) {
#pragma unroll
          for (int q = 0; q < 4; ++q) nb += __builtin_popcountll(__builtin_amdgcn_ballot_w64(kv[j][q] < kb));
          mx = max(mx, umax4(kv[j]));
        }
        if (nb >= 8 || __builtin_amdgcn_ballot_w64(mx == SENT) == 0ull) break;
        __builtin_amdgcn_s_sleep(2);
      }
      if (nb < 8) {
        asm volatile("s_waitcnt vmcnt(0)" ::: "memory");
#define PUSH(S, SLOT, KEY)                                                                                          \
  do {                                                                                                              \
    _Pragma("unroll") for (int y = 0; y < 2; ++y) __builtin_amdgcn_raw_buffer_store_b128(lv[S][y], rw, d0 * 2 + 256 * y, O_XS + (SLOT) * 2048, SC1); \
    _Pragma("unroll") for (int y = 0; y < 2; ++y) __builtin_amdgcn_raw_buffer_store_b128(hv[S][y], rw, d0 * 2 + 256 * y, O_XH + (SLOT) * 2048, SC1); \
    if (l == 0) __builtin_amdgcn_raw_buffer_store_b32(KEY, rw, 0, O_XK + (SLOT) * 128, SC1);                         \
  } while (0)
        if (kb == k0) { PUSH(0, nb, kb); sel0 = true; } else { PUSH(1, nb, kb); sel1 = true; }
        STAMP(3, 7);
        int ns = 0;
#pragma unroll
        for (int j = 0; j < 8; ++j)
#pragma unroll
          for (int q = 0; q < 4; ++q) ns += __builtin_popcountll(__builtin_amdgcn_ballot_w64(kv[j][q] < ks));
        if (ns < 8) {
          if (kb == k0) { PUSH(1, ns, ks); sel1 = true; } else { PUSH(0, ns, ks); sel0 = true; }
        }
#undef PUSH
      }
    }
    if (likely) {
      if (!sel0) {
#pragma unroll
        for (int y = 0; y < 2; ++y) __builtin_amdgcn_raw_buffer_store_b128(ov[0][y], ro, d0 * 2 + 256 * y, (b + 256 * w) * D_ * 2, AUX_OUT);
      }
      if (!sel1) {
#pragma unroll
        for (int y = 0; y < 2; ++y) __builtin_amdgcn_raw_buffer_store_b128(ov[1][y], ro, d0 * 2 + 256 * y, (b + 256 * (w + 4)) * D_ * 2, AUX_OUT);
      }
    }
  }
  STAMP(3, 0);
  bar();
  wait_until(Tk + D4);
  STAMP(3, 1);
  unsigned Ta;
  if (b < 224) {
    u4 xa[4];
    int np = 0;
    _Pragma("nounroll") for (int sp = 0; sp < SPIN_CAP; ++sp) {
      MEMB();
      ++np;
#pragma unroll
      for (int q = 0; q < 4; ++q) xa[q] = __builtin_amdgcn_raw_buffer_load_b128(rw, (r & 7) * 2048 + (r >> 3) * 256 + g * 16 + q * 64, O_XS + w * 512, SC1);
      if (__builtin_amdgcn_ballot_w64(max(max(umax4(xa[0]), umax4(xa[1])), max(umax4(xa[2]), umax4(xa[3]))) == SENT) == 0ull) break;
      __builtin_amdgcn_s_sleep(2);
    }
    STAMP(3, 2);
    COUNT(7, 2, 1000 + np);
    f4 g1 = z4, g2 = z4, u1 = z4, u2_ = z4;
#pragma unroll
    for (int q = 0; q < 4; ++q) {
      const LAS unsigned char* p = L + L_GU + w * 16384 + q * 2048 + l * 8;
      const u2 a0 = trread(p), a1 = trread(p + 512), c0 = trread(p + 1024), c1 = trread(p + 1536);
      const u2 e0 = trread(p + 8192), e1 = trread(p + 8704), f0 = trread(p + 9216), f1 = trread(p + 9728);
      g1 = mmb(u4{a0[0], a0[1], a1[0], a1[1]}, xa[q], g1);
      u1 = mmb(u4{c0[0], c0[1], c1[0], c1[1]}, xa[q], u1);
      g2 = mmb(u4{e0[0], e0[1], e1[0], e1[1]}, xa[q], g2);
      u2_ = mmb(u4{f0[0], f0[1], f1[0], f1[1]}, xa[q], u2_);
    }
    f4 cmb;
#pragma unroll
    for (int i = 0; i < 4; ++i) {
      const float gs = g1[i] + dppf<0x128>(g2[i]), us_ = u2_[i] + dppf<0x128>(u1[i]);
      cmb[i] = r < 8 ? gs : us_;
    }
    ((LAS f4*)(L + L_EX))[w * 64 + l] = cmb;
  }
  bar();
  if (b < 224 && w == 0) {
    const int tok = l & 7, fp = l >> 3;
    const LAS f2* rp = (const LAS f2*)(L + L_EX) + ((fp >> 1) * 16 + tok) * 2 + (fp & 1);
    const f2 gs = (rp[0] + rp[128]) + (rp[256] + rp[384]), us_ = (rp[16] + rp[144]) + (rp[272] + rp[400]);
    const unsigned gb = pkb(gs.x, gs.y), ub = pkb(us_.x, us_.y);
    const float g0 = blo(gb), g1 = bhi(gb);
    const unsigned sb = pkb(g0 * __builtin_amdgcn_rcpf(1.0f + __expf(-g0)), g1 * __builtin_amdgcn_rcpf(1.0f + __expf(-g1)));
    const unsigned ab = min(pkb(blo(sb) * blo(ub), bhi(sb) * bhi(ub)), SENT - 1u);
    __builtin_amdgcn_raw_buffer_store_b32(ab, rw, tok * 32 + fp * 4, O_ACT + fg * 256, SC1);
  }
  SB();
  Ta = b < 224 ? now32() + D5 : Tk + D4 + 70 + D5;
  STAMP(3, 3);
  const int tok = ki >> 1, col = nj * 64 + (ki & 1) * 32 + 4 * (l & 7) + 2 * ((l >> 4) & 1) + (l >> 5);
  {
    u4 wa[7];
#pragma unroll
    for (int ks = 0; ks < 7; ++ks) {
      const int sr = 4 * (g & 1) + (r >> 2);
      const LAS unsigned char* p = L + L_DN + ks * 4096 + (g >> 1) * 1024 + sr * 128 + ((w ^ (sr >> 1)) << 5) + (r & 3) * 8;
      const u2 a0 = trread(p), a1 = trread(p + 2048);
      wa[ks] = u4{a0[0], a0[1], a1[0], a1[1]};
      pin(wa[ks]);
    }
    wait_until(Ta);
    u4 xa[4];
    const int vo = ((r >> 3) * 6 + (g >> 1)) * 256 + (r & 7) * 32 + (g & 1) * 16;
    int np = 0;
    _Pragma("nounroll") for (int sp = 0; sp < SPIN_CAP; ++sp) {
      MEMB();
      ++np;
#pragma unroll
      for (int q = 0; q < 4; ++q) xa[q] = __builtin_amdgcn_raw_buffer_load_b128(rw, vo + q * 512, O_ACT + ki * 14 * 256, SC1);
      if (__builtin_amdgcn_ballot_w64(max(max(umax4(xa[0]), umax4(xa[1])), max(umax4(xa[2]), umax4(xa[3]))) == SENT) == 0ull) break;
      __builtin_amdgcn_s_sleep(1);
    }
    STAMP(3, 4);
    COUNT(7, 3, 1000 + np);
    f4 y1 = z4, y2 = z4;
#pragma unroll
    for (int q = 0; q < 4; ++q) {
      y1 = mmb(wa[q], xa[q], y1);
      if (q > 0) y2 = mmb(wa[q + 3], xa[q], y2);
    }
    u4 po;
#pragma unroll
    for (int i = 0; i < 4; ++i) po[i] = min(asu(y1[i] + dppf<0x128>(y2[i])), SENT - 1u);
    if (r < 8) __builtin_amdgcn_raw_buffer_store_b128(po, rw, r * 4096 + g * 16, O_PART + ki * 32768 + nj * 256 + w * 64, SC1);
  }
  if (w != 0) return;
  SB();
  const unsigned Tp = now32() + D6;
  STAMP(3, 5);
  {
    unsigned key, lnw, hw;
    _Pragma("nounroll") for (int sp = 0; sp < SPIN_CAP; ++sp) {
      MEMB();
      key = __builtin_amdgcn_raw_buffer_load_b32(rw, 0, O_XK + tok * 128, SC1);
      lnw = __builtin_amdgcn_raw_buffer_load_b32(rw, (col >> 1) * 4, O_XS + tok * 2048, SC1);
      hw = __builtin_amdgcn_raw_buffer_load_b32(rw, (col >> 1) * 4, O_XH + tok * 2048, SC1);
      if (__builtin_amdgcn_ballot_w64(umax3(key, lnw, hw) == SENT) == 0ull) break;
      __builtin_amdgcn_s_sleep(1);
    }
    const unsigned inv = key >> 16;
    const float rv = blo((inv & 0x8000u) ? inv : (inv ^ 0x7fffu));
    float gate = rbf(__builtin_amdgcn_rcpf(1.0f + __expf(-rv)));
    float lnf = (col & 1) ? bhi(lnw) : blo(lnw), hf = (col & 1) ? bhi(hw) : blo(hw);
    pin(gate); pin(lnf); pin(hf);
    wait_until(Tp);
    u4 p0, p1;
    const int vo = (l >> 3) * 32768 + (l & 7) * 16, so = O_PART + tok * 4096 + nj * 256 + (ki & 1) * 128;
    int np = 0;
    _Pragma("nounroll") for (int sp = 0; sp < SPIN_CAP; ++sp) {
      MEMB();
      ++np;
      p0 = __builtin_amdgcn_raw_buffer_load_b128(rw, vo, so, SC1);
      p1 = __builtin_amdgcn_raw_buffer_load_b128(rw, vo, so + 8 * 32768, SC1);
      if (__builtin_amdgcn_ballot_w64(max(umax4(p0), umax4(p1)) == SENT) == 0ull) break;
      __builtin_amdgcn_s_sleep(1);
    }
    COUNT(7, 4, 1000 + np);
    const float sa = swap32_add(asf(p0[0]) + asf(p1[0]), asf(p0[1]) + asf(p1[1])), sb = swap32_add(asf(p0[2]) + asf(p1[2]), asf(p0[3]) + asf(p1[3]));
    float y = swap16_add(sa, sb);
    y += dppf<0x128>(y);
    const float o = hf + rbf(lnf + rbf(rbf(y) * gate));
    if (r < 8) __builtin_amdgcn_raw_buffer_store_b16((us)(pkb(o, o) & 0xffffu), ro, col * 2 + (key & 2047u) * (D_ * 2), 0, AUX_OUT);
    STAMP(3, 6);
    DUMP();
  }
}

extern "C" void kernel_launch(void* const* d_in, const int* in_sizes, int n_in, void* d_out, int out_size, void* d_ws, size_t ws_size,
                              hipStream_t stream) {
  fused_kernel<<<256, 384, 0, stream>>>((const us*)d_in[0], (const us*)d_in[1], (const us*)d_in[2], (const us*)d_in[4], (const us*)d_in[5],
                                        (const us*)d_in[6], (const us*)d_in[7], (us*)d_out, (unsigned char*)d_ws + WS_BASE);
}
```

```cpp
#define KP 270
#define WE 0
#define DG 30
#include <hip/hip_runtime.h>
#include <stdint.h>

#define GAS __attribute__((address_space(1)))
#define LAS __attribute__((address_space(3)))
typedef _Float16 h2 __attribute__((ext_vector_type(2)));
typedef _Float16 h8 __attribute__((ext_vector_type(8)));
typedef __bf16 b2 __attribute__((ext_vector_type(2)));
typedef __bf16 b8 __attribute__((ext_vector_type(8)));
typedef float f2 __attribute__((ext_vector_type(2)));
typedef float f4 __attribute__((ext_vector_type(4)));
typedef unsigned u2 __attribute__((ext_vector_type(2)));
typedef unsigned u4 __attribute__((ext_vector_type(4)));
typedef short s4 __attribute__((ext_vector_type(4)));
typedef unsigned short us;
typedef __amdgpu_buffer_rsrc_t rsrc_t;

#define DEV __device__ __forceinline__
#define SB() __builtin_amdgcn_sched_barrier(0)

#define STAMP(K, I) do {} while (0)
#define COUNT(K, I, V) do {} while (0)
#define DUMP() do {} while (0)

#define C1 100
#define D1 50
#define D2 70
#define D3 40
#define D3U 90
#define D4 115
#define D5 40
#define D6 45
#define D7 150
#define N0 0
#define N1 0
#define LATE3 0
#define DG2 10
#define ROT 1
#define AUX_G 2
#define N2 32
#define N3 44
#define WPOS 1
#define AUX_IN 2
#define AUX_OUT 0
#define WS_MB 0
#define WBL2 0
#define DP 25
#define CPF 2
#define FSTR 128
constexpr int CODE_LEN = 22272;

constexpr int D_ = 1024, DF = 513, F_ = 3584;
constexpr int SC1 = 16;
constexpr unsigned SENT = 0xffffffffu;
constexpr int SPIN_CAP = 1 << 17;

constexpr int O_E = 0, O_FLAG = 4096, O_S = O_FLAG + 131072;
constexpr int O_KEY = O_S, O_XS = O_KEY + 32768, O_XH = O_XS + 16384, O_XK = O_XH + 16384;
constexpr int O_BLK = O_S + 73728, BLK = 16512, O_GN = O_BLK + 256 * BLK, WS_BYTES = O_GN + BLK;
constexpr int L_GU = 0, L_DN = 65536, L_Z = L_DN + 32768, L_EX = L_Z + 16448, L_CT = L_EX + 32768, L_BYTES = L_CT + 64
;
constexpr int YSTR = 1028;

DEV float asf(unsigned u) { return __builtin_bit_cast(float, u); }
DEV unsigned asu(float f) { return __builtin_bit_cast(unsigned, f); }
DEV unsigned pkh(float a, float b) { h2 v = {(_Float16)a, (_Float16)b}; return __builtin_bit_cast(unsigned, v); }
DEV unsigned pkb(float a, float b) { b2 v = {(__bf16)a, (__bf16)b}; return __builtin_bit_cast(unsigned, v); }
DEV float blo(unsigned u) { return asf(u << 16); }
DEV float bhi(unsigned u) { return asf(u & 0xffff0000u); }
DEV float rbf(float a) { return blo(pkb(a, a)); }
DEV f4 mmh(u4 a, u4 b, f4 c) { return __builtin_amdgcn_mfma_f32_16x16x32_f16(__builtin_bit_cast(h8, a), __builtin_bit_cast(h8, b), c, 0, 0, 0); }
DEV f4 mmb(u4 a, u4 b, f4 c) { return __builtin_amdgcn_mfma_f32_16x16x32_bf16(__builtin_bit_cast(b8, a), __builtin_bit_cast(b8, b), c, 0, 0, 0); }
DEV float cosr(float rev) { return __builtin_amdgcn_cosf(rev); }
DEV float sinr(float rev) { return __builtin_amdgcn_sinf(rev); }
DEV float dot2b(unsigned a, unsigned b, float c) { return __builtin_amdgcn_fdot2_f32_bf16(__builtin_bit_cast(b2, a), __builtin_bit_cast(b2, b), c, false); }
DEV f2 fma2(f2 a, f2 b, f2 c) { return __builtin_elementwise_fma(a, b, c); }
template <int CTRL> DEV float dppf(float x) { return asf((unsigned)__builtin_amdgcn_update_dpp(0, (int)asu(x), CTRL, 0xf, 0xf, true)); }
DEV float swap32_add(float a, float b) { auto s = __builtin_amdgcn_permlane32_swap(asu(a), asu(b), false, false); return asf(s[0]) + asf(s[1]); }
DEV float swap16_add(float a, float b) { auto s = __builtin_amdgcn_permlane16_swap(asu(a), asu(b), false, false); return asf(s[0]) + asf(s[1]); }
DEV float row_sum(float x) { x += dppf<0xB1>(x); x += dppf<0x4E>(x); x += dppf<0x124>(x); x += dppf<0x128>(x); return x; }
DEV float sum_wave(float x) { x = row_sum(x); x = swap16_add(x, x); return swap32_add(x, x); }
DEV unsigned now32() { return (unsigned)__builtin_amdgcn_s_memrealtime(); }
DEV void wait_until(unsigned t) { while ((int)(now32() - t) < 0) __builtin_amdgcn_s_sleep(1); }
DEV void bar() { asm volatile("s_waitcnt lgkmcnt(0)\n\ts_barrier" ::: "memory"); }
DEV void lds_arrive(int addr) {
  asm volatile("s_mov_b64 exec, 1\n\tds_add_u32 %0, %1\n\ts_mov_b64 exec, -1" ::"v"(addr), "v"(1) : "memory");
}
DEV void lds_wait(int addr, unsigned val) {
  unsigned cur;
  asm volatile("W%=:\n\tds_read_b32 %0, %1\n\ts_waitcnt lgkmcnt(0)\n\tv_cmp_le_u32 vcc, %2, %0\n\ts_cbranch_vccz W%=" : "=&v"(cur) : "v"(addr), "s"(val) : "memory", "vcc");
}
DEV void lds_wait_slow(int addr, unsigned val) {
  unsigned cur;
  asm volatile("W%=:\n\ts_sleep 1\n\tds_read_b32 %0, %1\n\ts_waitcnt lgkmcnt(0)\n\tv_cmp_le_u32 vcc, %2, %0\n\ts_cbranch_vccz W%=" : "=&v"(cur) : "v"(addr), "s"(val) : "memory", "vcc");
}
DEV void lbar(int addr, unsigned val) { lds_arrive(addr); lds_wait(addr, val); }
DEV void pin(u4& v) { asm volatile("" : "+v"(v)); }
DEV void pin(f2& v) { asm volatile("" : "+v"(v)); }
DEV void pin(f4& v) { asm volatile("" : "+v"(v)); }
DEV void pin(float& v) { asm volatile("" : "+v"(v)); }
DEV float hadd(f2 v) { float a = v.x; pin(a); return a + v.y; }
DEV unsigned umax3(unsigned a, unsigned b, unsigned c) { return max(max(a, b), c); }
DEV unsigned or4(u4 v) { return v[0] | v[1] | v[2] | v[3]; }
DEV unsigned and4(u4 v) { return v[0] & v[1] & v[2] & v[3]; }
DEV int pos_of(int a) { return (a & 15) * 16 + ((a >> 4) & 3) * 4 + (a >> 6); }
DEV int outbox(int a) { const int p = pos_of(a); return O_BLK + 128 * (p & 1) * BLK + (p >> 1) * 128; }
DEV unsigned umax4(u4 v) { return max(umax3(v[0], v[1], v[2]), v[3]); }
DEV u2 trread(const LAS void* p) { return __builtin_bit_cast(u2, __builtin_amdgcn_ds_read_tr16_b64_v4i16((LAS s4*)p)); }
DEV rsrc_t mkrs(const void* p, int bytes) { return __builtin_amdgcn_make_buffer_rsrc((void*)p, 0, bytes, 0x00020000); }
#define SGB(M, N) __builtin_amdgcn_sched_group_barrier(M, N, 0)
#define MFMA_VALU(REP, NV) _Pragma("unroll") for (int sg_ = 0; sg_ < REP; ++sg_) { SGB(0x8, 1); SGB(0x2, NV); }
DEV void pin(int& v) { asm volatile("" : "+v"(v)); }
#define MEMB() asm volatile("" ::: "memory")

DEV u4 cmul4(f4 re, f4 im, f2 a0, f2 b0, f2 a1, f2 b1) {
  const f2 yr0 = fma2(-im.xy, b0, re.xy * a0), yi0 = fma2(im.xy, a0, re.xy * b0);
  const f2 yr1 = fma2(-im.zw, b1, re.zw * a1), yi1 = fma2(im.zw, a1, re.zw * b1);
  return u4{pkh(yr0.x, yi0.x), pkh(yr0.y, yi0.y), pkh(yr1.x, yi1.x), pkh(yr1.y, yi1.y)};
}
DEV void ln_scalars(float mean, float var, float& mb, float& rs) {
  mb = rbf(mean);
  const float sd = rbf(__builtin_amdgcn_sqrtf(rbf(rbf(var) + 1e-5f)));
  rs = __builtin_amdgcn_rcpf(sd);
}
DEV int zaddr(int k) {
  const int k1 = k & 31, k2 = k >> 5;
  return k1 * 32 + ((((k2 >> 2) ^ (k1 >> 2)) & 7) << 2) + (k2 & 3);
}

struct Args { const us *x, *wrt, *wr, *wi, *wg, *wu, *wd; unsigned char* ws; us* out; };
__global__ __launch_bounds__(CPF >= 2 ? 448 : 384, 2) void fused_kernel(Args) {
  __shared__ __attribute__((aligned(16))) unsigned char sm[L_BYTES];
  unsigned cpf;
  const __attribute__((address_space(4))) Args* ka = (const __attribute__((address_space(4))) Args*)__builtin_amdgcn_kernarg_segment_ptr();
  if (CPF == 1) {
    unsigned long long pc0;
    const unsigned line = min((threadIdx.x >> 6) * 34 + min(threadIdx.x & 63, 33u), blockIdx.x < 64 ? (unsigned)(CODE_LEN / 128 - 1) : 0u) * 128;
    asm volatile("s_getpc_b64 %1\n\tglobal_load_dword %0, %3, %1 nt" : "=&v"(cpf), "=&s"(pc0), "+s"(ka) : "v"(line));
  } else {
    cpf = 0;
    if (CPF >= 2) {
      unsigned long long pc0;
      unsigned t0_, t1_;
      constexpr int STR = CPF == 2 ? 64 : 128;
      const unsigned slice = min((blockIdx.x >> 3) * (13 * STR), (unsigned)(CODE_LEN - 13 * STR));
      asm volatile("v_readfirstlane_b32 %1, %4\n\ts_getpc_b64 %0\n\ts_cmp_lt_u32 %1, 384\n\ts_cbranch_scc1 P%=\n\t"
                   "s_load_dword %2, %0, %5 offset:%c6*0\n\ts_load_dword %2, %0, %5 offset:%c6*1\n\ts_load_dword %2, %0, %5 offset:%c6*2\n\t"
                   "s_load_dword %2, %0, %5 offset:%c6*3\n\ts_load_dword %2, %0, %5 offset:%c6*4\n\ts_load_dword %2, %0, %5 offset:%c6*5\n\t"
                   "s_load_dword %2, %0, %5 offset:%c6*6\n\ts_load_dword %2, %0, %5 offset:%c6*7\n\ts_load_dword %2, %0, %5 offset:%c6*8\n\t"
                   "s_load_dword %2, %0, %5 offset:%c6*9\n\ts_load_dword %2, %0, %5 offset:%c6*10\n\ts_load_dword %2, %0, %5 offset:%c6*11\n\t"
                   "s_load_dword %2, %0, %5 offset:%c6*12\n\ts_load_dword %2, %3, 0x40\n\ts_barrier\n\ts_endpgm\nP%=:"
                   : "=&s"(pc0), "=&s"(t0_), "=&s"(t1_), "+s"(ka) : "v"(threadIdx.x), "s"(slice), "i"(STR) : "scc");
    }
  }
  const us *x = ka->x, *wrt = ka->wrt, *wr = ka->wr, *wi = ka->wi, *wg = ka->wg, *wu = ka->wu, *wd = ka->wd;
  unsigned char* ws = ka->ws;
  const int b = blockIdx.x, tid = threadIdx.x, l = tid & 63, w = __builtin_amdgcn_readfirstlane(tid >> 6);
  const int r = l & 15, g = l >> 4;
  LAS unsigned char* L = (LAS unsigned char*)sm;
  volatile LAS unsigned* ct = (volatile LAS unsigned*)(L + L_CT);
  const rsrc_t rw = mkrs(ws, WS_BYTES);
  const f4 z4 = {0.f, 0.f, 0.f, 0.f};
  const int cgp = (b & 7) * 7 + (b >> 5), kq = (b >> 3) & 3;

  STAMP(0, 0);
  const int mr = 8 * (r >> 2) + (r & 3);
  const int d0 = 32 * mr + 8 * g;
  u4 xv[2][2], wv[2];
  u2 ev;
  {
    const rsrc_t rx = mkrs(x, w < 4 ? 2048 * D_ * 2 : 0), rt = mkrs(wrt, w < 4 ? D_ * 2 : 0);
#pragma unroll
    for (int i = 0; i < 4; ++i)
      __builtin_amdgcn_raw_ptr_buffer_load_lds(rx, (LAS void*)(L + L_EX + w * 4096 + i * 1024), 16, l * 16, (b + 256 * w + 1024 * (i >> 1)) * D_ * 2 + (i & 1) * 1024, 0, AUX_IN);
#pragma unroll
    for (int y = 0; y < 2; ++y) wv[y] = __builtin_amdgcn_raw_buffer_load_b128(rt, d0 * 2 + 256 * y, 0, 0);
    ev = __builtin_amdgcn_raw_buffer_load_b64(rw, 0, O_E, SC1);
    asm volatile("ds_write_b32 %0, %1\n\ts_waitcnt lgkmcnt(0)\n\ts_barrier" ::"v"(L_CT + (l & 7) * 4), "v"(0), "s"(wr), "s"(wi), "s"(wg), "s"(wu), "s"(wd), "s"(ws) : "memory");
  }
  SB();
  const unsigned Tstart = now32();
  if (__builtin_expect(w == 4, 0)) {
    const rsrc_t rg = mkrs(wg, D_ * F_ * 2), ru = mkrs(wu, D_ * F_ * 2), rd = mkrs(wd, D_ * F_ * 2);
    const rsrc_t rr = mkrs(wr, 2048 * DF * 2), ri = mkrs(wi, 2048 * DF * 2);
    const int sr = l >> 3, rowl = 8 * (sr >> 2) + (sr & 3), chunk = ((l & 7) ^ (2 * (sr >> 1))) << 4;
    const int vgu = (256 * kq + rowl) * (F_ * 2) + cgp * 128 + chunk;
    const int vdn = (64 * cgp + rowl) * (D_ * 2) + kq * 512 + chunk;
    const int vw = (8 * (l >> 1) + (b & 7)) * (DF * 2) + (b >> 3) * 32 + (l & 1) * 16;
    int q = b < 224 ? 0 : 48;
    auto burst = [&](int n) {
      const int qe = min(q + n / 2, 48);
      _Pragma("nounroll") for (; q < qe; ++q) {
        if (q < 32) {
          const int so = (32 * (q >> 2) + 16 * (q & 1) + 4 * ((q >> 1) & 1)) * (F_ * 2);
          __builtin_amdgcn_raw_ptr_buffer_load_lds(rg, (LAS void*)(L + L_GU + q * 1024), 16, vgu, so, 0, AUX_IN);
          __builtin_amdgcn_raw_ptr_buffer_load_lds(ru, (LAS void*)(L + L_GU + 32768 + q * 1024), 16, vgu, so, 0, AUX_IN);
        } else {
          const int blk = (q - 32) >> 1, st = 2 * (q & 1);
          const int so = (32 * (blk >> 2) + 16 * (blk & 1) + 4 * ((blk >> 1) & 1)) * (D_ * 2) + st * 128;
          __builtin_amdgcn_raw_ptr_buffer_load_lds(rd, (LAS void*)(L + L_DN + (st * 8 + blk) * 1024), 16, vdn, so, 0, AUX_IN);
          __builtin_amdgcn_raw_ptr_buffer_load_lds(rd, (LAS void*)(L + L_DN + (st * 8 + 8 + blk) * 1024), 16, vdn, so + 128, 0, AUX_IN);
        }
      }
    };
    auto filter = [&](int lo, int hi) {
      _Pragma("nounroll") for (int i = lo; i < hi; ++i) {
        const int ii = (i + (WPOS ? ROT * (b >> 3) : 2 * ((b >> 3) & 3))) & 7;
        __builtin_amdgcn_raw_ptr_buffer_load_lds(rr, (LAS void*)(L + L_EX + 16384 + ii * 1024), 16, vw, ii * 256 * DF * 2, 0, AUX_G);
        __builtin_amdgcn_raw_ptr_buffer_load_lds(ri, (LAS void*)(L + L_EX + 24576 + ii * 1024), 16, vw, ii * 256 * DF * 2, 0, AUX_G);
      }
    };
    auto wait_word = [&](int word, unsigned val) { lds_wait_slow(L_CT + 4 * word, val); };
    STAMP(4, 0);
    if (DG > 0) wait_until(now32() + DG);
    if (WPOS >= 1) {
      const int row0 = (64 * (b >> 3) + (b & 7)) * (DF * 2);
      _Pragma("nounroll") for (int i = 0; i < 8; ++i) {
        __builtin_amdgcn_raw_ptr_buffer_load_lds(rr, (LAS void*)(L + L_DN), 16, l * 16, (row0 + i * 8 * DF * 2) & ~127, 0, AUX_IN);
        __builtin_amdgcn_raw_ptr_buffer_load_lds(ri, (LAS void*)(L + L_DN), 16, l * 16, (row0 + i * 8 * DF * 2) & ~127, 0, AUX_IN);
      }
      const int vt = ((row0 + (l >> 3) * 8 * DF * 2) & ~127) + 1024 + (l & 7) * 16;
      __builtin_amdgcn_raw_ptr_buffer_load_lds(rr, (LAS void*)(L + L_DN), 16, vt, 0, 0, AUX_IN);
      __builtin_amdgcn_raw_ptr_buffer_load_lds(ri, (LAS void*)(L + L_DN), 16, vt, 0, 0, AUX_IN);
      asm volatile("s_waitcnt vmcnt(0)" ::: "memory");
      STAMP(4, 1);
      if (DG2 > 0) wait_until(now32() + DG2);
      filter(0, WE);
      burst(N0);
    } else {
      filter(0, WE);
      burst(N0);
    }
    if (WE < 8 || N1 > 0) { wait_word(7, 4u); filter(WE, 8); burst(N1); }
    asm volatile("s_waitcnt vmcnt(0)\n\tds_write_b32 %0, %1" ::"v"((unsigned)(L_CT + 12)), "v"(1u) : "memory");
    STAMP(4, 2);
    wait_word(1, 4u);
    burst(N2);
    wait_word(1, 8u);
    burst(N3);
    if (q < 48) { wait_word(1, 12u); burst(96); }
    asm volatile("s_waitcnt vmcnt(0)\n\tds_write_b32 %0, %1" ::"v"((unsigned)(L_CT + 24)), "v"(1u) : "memory");
    STAMP(4, 5);
    asm volatile("" ::"v"(cpf), "v"(wv[0]), "v"(wv[1]), "v"(ev));
    DUMP();
    return;
  }

  if (__builtin_expect(w == 5, 0)) {
    const u4 ones = {SENT, SENT, SENT, SENT};
    if (l < 12) __builtin_amdgcn_raw_buffer_store_b128(ones, rw, l < 10 ? O_XS + b * 160 + l * 16 : O_KEY + b * 128 + (l & 1) * 16, 0, SC1);
    asm volatile("s_waitcnt vmcnt(0)" ::"v"(cpf) : "memory");
    lds_arrive(L_CT + 20);
    STAMP(6, 0);
    DUMP();
    if (b < 248) return;
    wait_until(now32() + 50);
    us nwr[4], nwi[4];
    u4 Fr, Fi, Ir, Ii;
    f2 tc[2], ts[2];
    {
      const rsrc_t rr = mkrs(wr, 2048 * DF * 2), ri = mkrs(wi, 2048 * DF * 2);
      const int vo = ((8 * (r + 64 * g) + (b & 7)) * DF + 512) * 2;
#pragma unroll
      for (int i = 0; i < 4; ++i) {
        nwr[i] = __builtin_amdgcn_raw_buffer_load_b16(rr, vo, i * 128 * DF * 2, 0);
        nwi[i] = __builtin_amdgcn_raw_buffer_load_b16(ri, vo, i * 128 * DF * 2, 0);
      }
#pragma unroll
      for (int i = 0; i < 4; ++i) {
        const int p = r * (4 * g + i);
        const float a = (float)(p & 15) * (1.0f / 16.0f);
        const float ca = cosr(a) * 0.5f, sa = sinr(a) * 0.5f;
        Fr[i] = pkh(ca, sa); Fi[i] = pkh(-sa, ca); Ir[i] = pkh(ca * 0.5f, -sa * 0.5f); Ii[i] = pkh(sa * 0.5f, ca * 0.5f);
        const float d = (float)p * (1.0f / 256.0f);
        tc[i >> 1][i & 1] = cosr(d); ts[i >> 1][i & 1] = sinr(d);
      }
    }
    lds_wait_slow(L_CT + 4, 4u);
    if (b == 255 && l == 0) {
      const unsigned long long e1 = (((unsigned long long)ev[1] << 32) | ev[0]) + 1ull;
      __builtin_amdgcn_raw_buffer_store_b64(u2{(unsigned)e1, (unsigned)(e1 >> 32)}, rw, 0, O_E, SC1);
    }
    u4 U;
#pragma unroll
    for (int i = 0; i < 4; ++i) U[i] = __builtin_amdgcn_raw_buffer_load_b32(rw, r * 1024 + g * 64 + i * 256, O_GN + (b & 7) * 4, SC1);
    f4 re = mmh(U, Fr, z4), im = mmh(U, Fi, z4);
    U = cmul4(re, im, tc[0], -ts[0], tc[1], -ts[1]);
    re = mmh(Fr, U, z4); im = mmh(Fi, U, z4);
    U = cmul4(re, im, f2{blo(nwr[0]), blo(nwr[1])}, f2{blo(nwi[0]), blo(nwi[1])}, f2{blo(nwr[2]), blo(nwr[3])}, f2{blo(nwi[2]), blo(nwi[3])});
    re = mmh(U, Ir, z4); im = mmh(U, Ii, z4);
    U = cmul4(re, im, tc[0], ts[0], tc[1], ts[1]);
    re = mmh(Ir, U, z4); im = mmh(Ii, U, z4);
#pragma unroll
    for (int i = 0; i < 4; ++i) __builtin_amdgcn_raw_buffer_store_b32(pkh(re[i], im[i]) | 1u, rw, r * 1024 + g * 64 + i * 256, O_GN + (b & 7) * 4, SC1);
    return;
  }

  const unsigned odd = (r & 1) ? 0x80008000u : 0u;
  const float rf = (float)r, g4 = (float)(4 * g);
  int gvo = ((r >> 2) * 8 + 2 * g) * BLK + (r & 3) * 16;
  const int gso = O_BLK + 64 * w * BLK + pos_of(b) * 64;
  pin(gvo);
  {
    u4 Cm[2], Sm[2], Nm[2];
    f2 twc[2][4], tws[2][4];
    {
      const float d32 = rf * (1.0f / 32.0f);
#pragma unroll
      for (int q = 0; q < 2; ++q) {
        const float a0 = (g4 + (float)(2 * q)) * d32, a1 = a0 + d32;
        Cm[0][q] = pkh(cosr(a0), cosr(a1)); Cm[0][q + 2] = Cm[0][q] ^ odd;
        Sm[0][q] = pkh(sinr(a0), sinr(a1)); Sm[0][q + 2] = Sm[0][q] ^ odd;
      }
      Nm[0] = Sm[0] ^ 0x80008000u; Cm[1] = Cm[0] ^ 0x80000000u; Sm[1] = Sm[0] ^ 0x80000000u; Nm[1] = Sm[0] ^ 0x00008000u;
#pragma unroll
      for (int t = 0; t < 2; ++t) {
        pin(Cm[t]); pin(Sm[t]); pin(Nm[t]);
        const float dk = (rf + (float)(16 * t)) * (1.0f / 1024.0f);
#pragma unroll
        for (int ta = 0; ta < 2; ++ta) {
          float a = (g4 + (float)(16 * ta)) * dk;
#pragma unroll
          for (int j = 0; j < 4; ++j) {
            twc[t][2 * ta + (j >> 1)][j & 1] = cosr(a); tws[t][2 * ta + (j >> 1)][j & 1] = sinr(a);
            a += dk;
          }
        }
#pragma unroll
        for (int q = 0; q < 4; ++q) { pin(twc[t][q]); pin(tws[t][q]); }
      }
    }
    u4 MA, MB;
    {
      const int rho = r >> 1, part = r & 1;
      const int base = (b + 256 * g) * rho + 512 * part;
      const int e = (rho & 1) ? -256 : 256;
      const float aa = (float)((base + e) & 2047) * (1.0f / 2048.0f), ab = (float)((base - e) & 2047) * (1.0f / 2048.0f);
      const float sc = 0.70710678118f / 64.0f;
      const unsigned a0 = pkh(cosr(aa) * sc, sinr(aa) * sc), a1 = pkh(cosr(ab) * sc, -sinr(ab) * sc);
      MA = u4{a0, a1, 0u, 0u};
      MB = u4{0u, 0u, a0, a1};
      pin(MA); pin(MB);
    }
    int xa_ = L_EX + w * 4096 + (4 * g + (r >> 2)) * 64 + (r & 3) * 8, xb_ = L_EX + w * 4096 + d0 * 2;
    int zf = L_Z + (w * YSTR + 2 * (r + 128 * g)) * 4, zm = L_Z + (w * YSTR + 2 * (400 - r - 128 * g) + 1) * 4, zr_ = L_Z + (g * YSTR + 2 * (128 * w + 4 * r)) * 4;
    int z0 = l == 0 ? L_Z + (w * YSTR + 1) * 4 : zf;
    pin(xa_); pin(xb_); pin(zf); pin(zm); pin(zr_); pin(z0);
    SB();
    STAMP(0, 1);
    asm volatile("s_waitcnt vmcnt(3)" ::"v"(cpf) : "memory");
    STAMP(0, 2);
    u4 X[2][2];
#pragma unroll
    for (int s_ = 0; s_ < 2; ++s_)
#pragma unroll
      for (int ta = 0; ta < 2; ++ta) {
        const u2 a0 = trread(L + xa_ + s_ * 2048 + ta * 32), a1 = trread(L + xa_ + s_ * 2048 + ta * 32 + 1024);
        X[s_][ta] = u4{a0[0], a0[1], a1[0], a1[1]};
      }
#pragma unroll
    for (int s_ = 0; s_ < 2; ++s_)
#pragma unroll
      for (int y = 0; y < 2; ++y) xv[s_][y] = *(const LAS u4*)(L + xb_ + s_ * 2048 + y * 256);
    float s1[2] = {0.f, 0.f}, s2[2] = {0.f, 0.f};
#pragma unroll
    for (int ta = 0; ta < 2; ++ta)
#pragma unroll
      for (int q = 0; q < 4; ++q)
#pragma unroll
        for (int s_ = 0; s_ < 2; ++s_) { s1[s_] = dot2b(X[s_][ta][q], 0x3f803f80u, s1[s_]); s2[s_] = dot2b(X[s_][ta][q], X[s_][ta][q], s2[s_]); }
    f2 xf[2][2][4];
#pragma unroll
    for (int s_ = 0; s_ < 2; ++s_)
#pragma unroll
      for (int ta = 0; ta < 2; ++ta)
#pragma unroll
        for (int q = 0; q < 4; ++q) xf[s_][ta][q] = f2{blo(X[s_][ta][q]), bhi(X[s_][ta][q])};
    const float t12 = row_sum(swap16_add(swap32_add(s1[0], s1[1]), swap32_add(s2[0], s2[1])));
    auto sw = __builtin_amdgcn_permlane16_swap(asu(t12), asu(t12), false, false);
    const float mean = asf(sw[0]) * (1.0f / 1024.0f);
    float var = __builtin_fmaf(asf(sw[1]), 1.0f / 1024.0f, -mean * mean);
#pragma unroll
    for (int s_ = 0; s_ < 2; ++s_)
#pragma unroll
      for (int ta = 0; ta < 2; ++ta)
#pragma unroll
        for (int q = 0; q < 4; ++q) pin(xf[s_][ta][q]);
    if (__builtin_expect(__builtin_amdgcn_ballot_w64(mean * mean > 16.0f * var) != 0ull, 0)) {
      float qs[2];
#pragma unroll
      for (int s_ = 0; s_ < 2; ++s_) {
        const float m = asf(__builtin_amdgcn_readlane(asu(mean), 32 * s_));
        f2 acc = {0.f, 0.f};
#pragma unroll
        for (int j = 0; j < 8; ++j) { const f2 d = xf[s_][j >> 2][j & 3] - f2{m, m}; acc = fma2(d, d, acc); }
        qs[s_] = hadd(acc);
      }
      const float q = swap32_add(qs[0], qs[1]);
      var = row_sum(swap16_add(q, q)) * (1.0f / 1024.0f);
    }
    float mb, rs;
    ln_scalars(mean, var, mb, rs);
    rs *= 0.125f;
    const float off = -mb * rs;
#pragma unroll
    for (int s_ = 0; s_ < 2; ++s_) {
      const float rs_ = asf(__builtin_amdgcn_readlane(asu(rs), 32 * s_)), of_ = asf(__builtin_amdgcn_readlane(asu(off), 32 * s_));
#pragma unroll
      for (int j = 0; j < 8; ++j) xf[s_][j >> 2][j & 3] = fma2(xf[s_][j >> 2][j & 3], f2{rs_, rs_}, f2{of_, of_});
    }
    SB();
    f4 vr[2][2], vi[2][2];
#pragma unroll
    for (int ta = 0; ta < 2; ++ta) {
#pragma unroll
      for (int s_ = 0; s_ < 2; ++s_)
#pragma unroll
        for (int q = 0; q < 4; ++q) X[s_][ta][q] = pkh(xf[s_][ta][q].x, xf[s_][ta][q].y);
#pragma unroll
      for (int t = 0; t < 2; ++t) {
        vr[t][ta] = mmh(X[1][ta], Sm[t], mmh(X[0][ta], Cm[t], z4));
        vi[t][ta] = mmh(X[1][ta], Cm[t], mmh(X[0][ta], Nm[t], z4));
      }
    }
    SGB(0x2, 4); MFMA_VALU(16, 2)
    SB();
    f2 tr[2][4], ti[2][4];
#pragma unroll
    for (int t = 0; t < 2; ++t)
#pragma unroll
      for (int q = 0; q < 4; ++q) {
        const f2 xr = (q & 1) ? vr[t][q >> 1].zw : vr[t][q >> 1].xy, xi = (q & 1) ? vi[t][q >> 1].zw : vi[t][q >> 1].xy;
        tr[t][q] = fma2(xi, tws[t][q], xr * twc[t][q]);
        ti[t][q] = fma2(-xr, tws[t][q], xi * twc[t][q]);
      }
    SB();
#pragma unroll
    for (int t = 0; t < 2; ++t) {
      u4 Vr, Vi;
#pragma unroll
      for (int q = 0; q < 4; ++q) { Vr[q] = pkh(tr[t][q].x, tr[t][q].y); Vi[q] = pkh(ti[t][q].x, ti[t][q].y); }
#pragma unroll
      for (int u = 0; u < 2; ++u) {
        const f4 zr = mmh(Sm[u], Vi, mmh(Cm[u], Vr, z4)), zi = mmh(Cm[u], Vi, mmh(Nm[u], Vr, z4));
#pragma unroll
        for (int i = 0; i < 4; ++i) {
          const unsigned d = pkh(zr[i], zi[i]);
          if (u == 0) *(LAS unsigned*)(L + zf + (16 * t + 32 * i) * 8) = d;
          else *(LAS unsigned*)(L + zm + (112 - 16 * t - 32 * i) * 8) = d;
          if (t == 0 && u == 0 && i == 0) *(LAS unsigned*)(L + z0) = d;
        }
      }
    }
    SGB(0x2, 4); MFMA_VALU(8, 2)
#pragma unroll
    for (int sg_ = 0; sg_ < 8; ++sg_) { SGB(0x8, 1); SGB(0x2, 2); SGB(0x200, 1); }
    SB();
    STAMP(0, 3);
    lbar(L_CT + 20, 5u);
    STAMP(0, 4);
    u4 og[2][2], F[2][2];
#pragma unroll
    for (int T = 0; T < 2; ++T) { F[T][0] = *(const LAS u4*)(L + zr_ + T * 512); F[T][1] = *(const LAS u4*)(L + zr_ + T * 512 + 16); }
#pragma unroll
    for (int T = 0; T < 2; ++T) {
      const f4 o0 = mmh(MA, F[T][0], z4), o1 = mmh(MB, F[T][0], z4), o2 = mmh(MA, F[T][1], z4), o3 = mmh(MB, F[T][1], z4);
#pragma unroll
      for (int a = 0; a < 2; ++a) {
        og[T][a] = u4{pkh(o0[2 * a], o0[2 * a + 1]), pkh(o1[2 * a], o1[2 * a + 1]), pkh(o2[2 * a], o2[2 * a + 1]), pkh(o3[2 * a], o3[2 * a + 1])} & 0xfffffffeu;
        __builtin_amdgcn_raw_buffer_store_b128(og[T][a], rw, gvo, gso + (32 * T + a) * BLK, SC1);
      }
    }
    SGB(0x100, 4); SGB(0x8, 3); MFMA_VALU(5, 3)
    asm volatile("s_nop 1" ::"v"(og[0][0]), "v"(og[0][1]), "v"(og[1][0]), "v"(og[1][1]));
    if (w == 3) {
      const unsigned zn = *(const LAS unsigned*)(L + L_Z + (g * YSTR + 1025) * 4);
      const f4 o = mmh(MA, u4{zn, zn, zn, zn}, z4);
      if (r == 0) __builtin_amdgcn_raw_buffer_store_b64(u2{pkh(o[0], o[1]), pkh(o[2], o[3])} & 0xfffffffeu, rw, g * 8, O_GN + pos_of(b) * 64, SC1);
    }
    SB();
    STAMP(0, 5);
  }
  unsigned T2;
  us* out;
  {
    SB();
    u4 M1r[4], M1i[4], M2r[4], M2i[4], Fr, Fi, Ir, Ii;
    const float w4 = (float)(4 * (tid >> 6));
    {
      const float d = rf * (1.0f / 256.0f), d16 = rf * (1.0f / 16.0f);
#pragma unroll
      for (int e = 0; e < 4; ++e) {
        float a = (16.0f * g4 + w4 + (float)e) * d;
#pragma unroll
        for (int i = 0; i < 4; ++i) {
          const float ca = cosr(a), sa = sinr(a);
          M1r[e][i] = pkh(ca, sa); M1i[e][i] = pkh(-sa, ca);
          a += d16;
        }
        pin(M1r[e]); pin(M1i[e]);
      }
#pragma unroll
      for (int i = 0; i < 4; ++i) {
        const float a = (g4 + (float)i) * d16;
        const float ca = cosr(a) * 0.25f, sa = sinr(a) * 0.25f;
        Fr[i] = pkh(ca, sa); Fi[i] = pkh(-sa, ca); Ir[i] = pkh(ca * 0.25f, -sa * 0.25f); Ii[i] = pkh(sa * 0.25f, ca * 0.25f);
      }
      pin(Fr); pin(Fi); pin(Ir); pin(Ii);
    }
    int exw = L_EX + (g * 256 + w * 16 + r) * 16, exr = L_EX + (w * 256 + l) * 16;
    pin(exw); pin(exr);
    f2 wa[4][2], wb[4][2];
    auto read_filter = [&]() {
    lds_wait(L_CT + 12, 1u);
#pragma unroll
    for (int i1 = 0; i1 < 4; ++i1) {
      const LAS unsigned char* p = L + L_EX + 16384 + (4 * w + i1 + 64 * g + 16 * (r >> 2)) * 32 + (r & 3) * 8;
      const u2 a = trread(p), c = trread(p + 8192);
#pragma unroll
      for (int h = 0; h < 2; ++h) {
        wa[i1][h] = f2{blo(a[h]), bhi(a[h])};
        wb[i1][h] = f2{blo(c[h]), bhi(c[h])};
        pin(wa[i1][h]); pin(wb[i1][h]);
      }
    }
    };
    out = ka->out;
    asm volatile("" : "+s"(out));
    SB();
    STAMP(1, 0);
    const unsigned long long e1 = (((unsigned long long)ev[1] << 32) | ev[0]) + 1ull;
    const u2 tag = {(unsigned)e1, (unsigned)(e1 >> 32)};
    asm volatile("s_waitcnt vmcnt(0)" ::: "memory");
    if (l == 0) __builtin_amdgcn_raw_buffer_store_b64(tag, rw, 0, O_FLAG + (w * 256 + pos_of(b)) * 128, SC1);
    SB();
    lds_arrive(L_CT + 28);
    const unsigned Tf = now32() + D1;
    STAMP(1, 7);
#pragma unroll
    for (int e = 0; e < 4; ++e) {
      const float d = (16.0f * rf + w4 + (float)e) * (1.0f / 256.0f);
      float a = g4 * d;
#pragma unroll
      for (int i = 0; i < 4; ++i) {
        const float cd = cosr(a), sd = sinr(a);
        M2r[e][i] = pkh(cd, -sd); M2i[e][i] = pkh(sd, cd);
        a += d;
      }
      pin(M2r[e]); pin(M2i[e]);
    }
    SB();
    wait_until(Tf);
    wait_until(Tstart + KP);
    int np = 0;
    {
      bool miss = true;
      _Pragma("nounroll") for (int sp = 0; sp < SPIN_CAP; ++sp) {
        MEMB();
        if (miss) {
          const u2 f = __builtin_amdgcn_raw_buffer_load_b64(rw, l * 128, O_FLAG + ((b >> 6) * 256 + w * 64) * 128, SC1);
          miss = ((f[0] ^ tag[0]) | (f[1] ^ tag[1])) != 0u;
        }
        if (__builtin_amdgcn_ballot_w64(miss) == 0ull) break;
        ++np;
        __builtin_amdgcn_s_sleep(1);
      }
      MEMB();
    }
    STAMP(1, 1);
    u4 B[4];
    const int so = O_BLK + b * BLK + w * 4096;
#pragma unroll
    for (int e = 0; e < 4; ++e)
#pragma unroll
      for (int i = 0; i < 4; ++i) B[e][i] = __builtin_amdgcn_raw_buffer_load_b32(rw, l * 4 + (e * 4 + i) * 256, so, SC1);
    SB();
    lds_arrive(L_CT + 4);
    SB();
    read_filter();
    SB();
    SB();
    STAMP(1, 2);
    STAMP(1, 3);
    COUNT(7, 0, 1000 + np);
    {
      u4 T[4];
#pragma unroll
      for (int e = 0; e < 4; ++e) {
        const f4 re = mmh(M1r[e], B[e], z4), im = mmh(M1i[e], B[e], z4);
#pragma unroll
        for (int i = 0; i < 4; ++i) T[i][e] = pkh(re[i], im[i]);
      }
#pragma unroll
      for (int i = 0; i < 4; ++i) *(LAS u4*)(L + exw + i * 1024) = T[i];
      SGB(0x8, 4); MFMA_VALU(4, 2)
    }
    SB();
    lbar(L_CT + 20, 9u);
    STAMP(1, 5);
    {
      u4 Y[4], U[4];
      f4 re[4], im[4];
#pragma unroll
      for (int i1 = 0; i1 < 4; ++i1) Y[i1] = *(const LAS u4*)(L + exr + i1 * 1024);
#pragma unroll
      for (int i1 = 0; i1 < 4; ++i1) { re[i1] = mmh(Fr, Y[i1], z4); im[i1] = mmh(Fi, Y[i1], z4); }
      SB();
      f2 yr[4][2], yi[4][2];
#pragma unroll
      for (int i1 = 0; i1 < 4; ++i1) {
        yr[i1][0] = fma2(-im[i1].xy, wb[i1][0], re[i1].xy * wa[i1][0]); yi[i1][0] = fma2(im[i1].xy, wa[i1][0], re[i1].xy * wb[i1][0]);
        yr[i1][1] = fma2(-im[i1].zw, wb[i1][1], re[i1].zw * wa[i1][1]); yi[i1][1] = fma2(im[i1].zw, wa[i1][1], re[i1].zw * wb[i1][1]);
      }
      SB();
#pragma unroll
      for (int i1 = 0; i1 < 4; ++i1) {
        Y[i1] = u4{pkh(yr[i1][0].x, yi[i1][0].x), pkh(yr[i1][0].y, yi[i1][0].y), pkh(yr[i1][1].x, yi[i1][1].x), pkh(yr[i1][1].y, yi[i1][1].y)};
        const f4 r2 = mmh(Ir, Y[i1], z4), i2v = mmh(Ii, Y[i1], z4);
#pragma unroll
        for (int e = 0; e < 4; ++e) U[e][i1] = pkh(r2[e], i2v[e]);
      }
#pragma unroll
      for (int e = 0; e < 4; ++e) *(LAS u4*)(L + exw + 16384 + e * 1024) = U[e];
      SGB(0x2, 4); MFMA_VALU(8, 3)
    }
    SB();
    lbar(L_CT + 20, 13u);
    STAMP(1, 6);
    {
      u4 Xe[4];
#pragma unroll
      for (int e = 0; e < 4; ++e) Xe[e] = *(const LAS u4*)(L + exr + 16384 + e * 1024);
#pragma unroll
      for (int e = 0; e < 4; ++e) {
        const f4 re = mmh(M2r[e], Xe[e], z4), im = mmh(M2i[e], Xe[e], z4);
#pragma unroll
        for (int i = 0; i < 4; ++i) __builtin_amdgcn_raw_buffer_store_b32(pkh(re[i], im[i]) | 1u, rw, l * 4 + (e * 4 + i) * 256, so, SC1);
      }
      SGB(0x100, 4); SGB(0x8, 4);
#pragma unroll
      for (int sg_ = 0; sg_ < 4; ++sg_) { SGB(0x2, 4); SGB(0x40, 2); SGB(0x8, 1); }
    }
    SB();
    T2 = now32() + D2;
    STAMP(1, 4);
  }
  f4 hp[2][2], hq[2][2];
  f2 wf[2][4];
  float wsum, wthr;
  {
    u4 MA, MB;
    {
      const int ww = r >> 2, i = r & 3;
      unsigned cf[2];
#pragma unroll
      for (int a = 0; a < 2; ++a) {
        const int e = ((i & 2) ? -1 : 1) * ((a ? -256 : 256) - 512 * (i & 1));
        const float an = (float)(((b + 256 * ww) * (2 * g + a) + e) & 2047) * (1.0f / 2048.0f);
        const float sc = 0.70710678118f / 32.0f;
        cf[a] = pkh(cosr(an) * sc, -sinr(an) * sc);
      }
      MA = u4{cf[0], 0u, cf[1], 0u};
      MB = u4{0u, cf[0], 0u, cf[1]};
      pin(MA); pin(MB);
    }
    u4 Br[2][2], Bi[2][2], Cm[2], Sm[2], Nm[2];
    const float mrf = (float)mr;
#pragma unroll
    for (int vv = 0; vv < 2; ++vv) {
      float c[4], s[4];
      const float d = (mrf + (float)(4 * vv)) * (1.0f / 32.0f);
      float a = g4 * d;
#pragma unroll
      for (int j = 0; j < 4; ++j) {
        c[j] = cosr(a); s[j] = sinr(a);
        Br[vv][0][j] = pkh(c[j], -s[j]); Br[vv][1][j] = Br[vv][0][j] ^ odd;
        Bi[vv][0][j] = pkh(s[j], c[j]); Bi[vv][1][j] = Bi[vv][0][j] ^ odd;
        a += d;
      }
#pragma unroll
      for (int q = 0; q < 2; ++q) {
        Cm[vv][q] = pkh(c[2 * q], c[2 * q + 1]); Cm[vv][q + 2] = Cm[vv][q] ^ odd;
        Sm[vv][q] = pkh(s[2 * q], s[2 * q + 1]); Sm[vv][q + 2] = Sm[vv][q] ^ odd;
        Nm[vv][q] = Sm[vv][q] ^ 0x80008000u; Nm[vv][q + 2] = Sm[vv][q + 2] ^ 0x80008000u;
      }
      pin(Br[vv][0]); pin(Br[vv][1]); pin(Bi[vv][0]); pin(Bi[vv][1]); pin(Cm[vv]); pin(Sm[vv]); pin(Nm[vv]);
    }
    f2 twc[2][4], tws[2][4];
#pragma unroll
    for (int vv = 0; vv < 2; ++vv) {
      const float d = (mrf + (float)(4 * vv)) * (1.0f / 1024.0f);
#pragma unroll
      for (int t = 0; t < 2; ++t) {
        float a = (g4 + (float)(16 * t)) * d;
#pragma unroll
        for (int j = 0; j < 4; ++j) {
          twc[vv][2 * t + (j >> 1)][j & 1] = cosr(a); tws[vv][2 * t + (j >> 1)][j & 1] = sinr(a);
          a += d;
        }
        pin(twc[vv][2 * t]); pin(tws[vv][2 * t]); pin(twc[vv][2 * t + 1]); pin(tws[vv][2 * t + 1]);
      }
    }
    SB();
    STAMP(2, 0);
    wait_until(T2);
    STAMP(2, 1);
    u4 Lq[2][2];
    u2 Ln = {1u, 1u};
#pragma unroll
    for (int T = 0; T < 2; ++T)
#pragma unroll
      for (int a = 0; a < 2; ++a) Lq[T][a] = __builtin_amdgcn_raw_buffer_load_b128(rw, gvo, gso + (32 * T + a) * BLK, SC1);
    if (w == 3) Ln = __builtin_amdgcn_raw_buffer_load_b64(rw, g * 8, O_GN + pos_of(b) * 64, SC1);
    SB();
    if (!LATE3) lds_arrive(L_CT + 4);
    SB();
    float ws1 = 0.f, ws2 = 0.f;
#pragma unroll
    for (int y = 0; y < 2; ++y) {
#pragma unroll
      for (int vv = 0; vv < 2; ++vv) {
        hp[y][vv] = f4{blo(xv[0][y][2 * vv]), bhi(xv[0][y][2 * vv]), blo(xv[0][y][2 * vv + 1]), bhi(xv[0][y][2 * vv + 1])};
        hq[y][vv] = f4{blo(xv[1][y][2 * vv]), bhi(xv[1][y][2 * vv]), blo(xv[1][y][2 * vv + 1]), bhi(xv[1][y][2 * vv + 1])};
        pin(hp[y][vv]); pin(hq[y][vv]);
      }
#pragma unroll
      for (int q = 0; q < 4; ++q) {
        wf[y][q] = f2{blo(wv[y][q]), bhi(wv[y][q])};
        pin(wf[y][q]);
        ws1 = dot2b(wv[y][q], 0x3f803f80u, ws1); ws2 = dot2b(wv[y][q], wv[y][q], ws2);
      }
    }
    wsum = sum_wave(ws1);
    wthr = 2.0f * __builtin_amdgcn_sqrtf(sum_wave(ws2));
    pin(wsum); pin(wthr);
    const int kb0 = 128 * w + 4 * r;
    int za = L_Z + (g * YSTR + zaddr(kb0)) * 4, zb = L_Z + (g * YSTR + zaddr(1024 - 64 - 3 - kb0)) * 4;
    int zc0 = L_Z + (g * YSTR + (kb0 == 0 ? 1024 : zaddr(1024 - kb0))) * 4, zc1 = L_Z + (g * YSTR + zaddr(1024 - 64 - kb0)) * 4;
    int zq[2][2];
#pragma unroll
    for (int t = 0; t < 2; ++t)
#pragma unroll
      for (int u = 0; u < 2; ++u) { zq[t][u] = L_Z + (w * YSTR + zaddr(16 * t + r + 32 * (16 * u + 4 * g))) * 4; pin(zq[t][u]); }
    pin(za); pin(zb); pin(zc0); pin(zc1);
    SB();
    STAMP(2, 2);
    int np = 0;
    _Pragma("nounroll") for (int sp = 0; sp < SPIN_CAP; ++sp) {
      if (__builtin_amdgcn_ballot_w64(((and4(Lq[0][0]) & and4(Lq[0][1]) & and4(Lq[1][0]) & and4(Lq[1][1]) & Ln[0] & Ln[1]) & 1u) == 0u) == 0ull) break;
      MEMB();
      ++np;
#pragma unroll
      for (int T = 0; T < 2; ++T)
#pragma unroll
        for (int a = 0; a < 2; ++a) Lq[T][a] = __builtin_amdgcn_raw_buffer_load_b128(rw, gvo, gso + (32 * T + a) * BLK, SC1);
      if (w == 3) Ln = __builtin_amdgcn_raw_buffer_load_b64(rw, g * 8, O_GN + pos_of(b) * 64, SC1);
    }
    if (LATE3) lds_arrive(L_CT + 4);
    STAMP(2, 3);
    COUNT(7, 1, 1000 + np);
#pragma unroll
    for (int T = 0; T < 2; ++T) {
      const u4 F0 = {Lq[T][0][0], Lq[T][0][1], Lq[T][1][0], Lq[T][1][1]}, F1 = {Lq[T][0][2], Lq[T][0][3], Lq[T][1][2], Lq[T][1][3]};
      f4 o[4] = {mmh(MA, F0, z4), mmh(MB, F0, z4), mmh(MA, F1, z4), mmh(MB, F1, z4)};
      if (T == 0) {
        o[0][0] = kb0 == 0 ? 0.5f * (o[0][0] + o[0][2]) : o[0][0];
        o[0][1] = kb0 == 0 ? 0.5f * (o[0][1] + o[0][3]) : o[0][1];
      }
#pragma unroll
      for (int xx = 0; xx < 4; ++xx) {
        *(LAS unsigned*)(L + za + (32 * xx + 2 * T) * 4) = pkh(o[xx][0], o[xx][1]);
        *(LAS unsigned*)(L + (xx == 0 ? (T ? zc1 : zc0) : zb + (32 * (3 - xx) + 2 * (1 - T)) * 4)) = pkh(o[xx][2], o[xx][3]);
      }
    }
    SB();
    if (w == 3) {
      const f4 o = mmh(MA, u4{Ln[0], Ln[0], Ln[1], Ln[1]}, z4);
      if (r == 0) ((LAS unsigned*)(L + L_Z))[g * YSTR + zaddr(512)] = pkh(0.5f * (o[0] + o[2]), 0.5f * (o[1] + o[3]));
    }
    lbar(L_CT + 20, 17u);
    STAMP(2, 6);
    u4 Az[2][2];
#pragma unroll
    for (int t = 0; t < 2; ++t)
#pragma unroll
      for (int u = 0; u < 2; ++u) Az[t][u] = *(const LAS u4*)(L + zq[t][u]);
    f4 re[2][2], im[2][2];
#pragma unroll
    for (int t = 0; t < 2; ++t)
#pragma unroll
      for (int vv = 0; vv < 2; ++vv) {
        re[vv][t] = mmh(Az[t][1], Br[vv][1], mmh(Az[t][0], Br[vv][0], z4));
        im[vv][t] = mmh(Az[t][1], Bi[vv][1], mmh(Az[t][0], Bi[vv][0], z4));
      }
    SB();
    f2 ta[2][4], tb[2][4];
#pragma unroll
    for (int vv = 0; vv < 2; ++vv)
#pragma unroll
      for (int q = 0; q < 4; ++q) {
        const f2 xr = (q & 1) ? re[vv][q >> 1].zw : re[vv][q >> 1].xy, xi = (q & 1) ? im[vv][q >> 1].zw : im[vv][q >> 1].xy;
        ta[vv][q] = fma2(-xi, tws[vv][q], xr * twc[vv][q]);
        tb[vv][q] = fma2(xi, twc[vv][q], xr * tws[vv][q]);
      }
    SB();
#pragma unroll
    for (int vv = 0; vv < 2; ++vv) {
      u4 Tr, Ti;
#pragma unroll
      for (int q = 0; q < 4; ++q) { Tr[q] = pkh(ta[vv][q].x, ta[vv][q].y); Ti[q] = pkh(tb[vv][q].x, tb[vv][q].y); }
#pragma unroll
      for (int y = 0; y < 2; ++y) {
        hp[y][vv] = mmh(Ti, Nm[y], mmh(Tr, Cm[y], hp[y][vv]));
        hq[y][vv] = mmh(Ti, Cm[y], mmh(Tr, Sm[y], hq[y][vv]));
      }
    }
    SGB(0x2, 4); MFMA_VALU(16, 2)
    SB();
  }
  STAMP(2, 4);
  unsigned k0, k1, Tk;
  bool likely;
  u4 lv[2][2], hv[2][2], ov[2][2];
  {
    f2 a1[2] = {{0.f, 0.f}, {0.f, 0.f}}, a2[2] = {{0.f, 0.f}, {0.f, 0.f}}, a3[2] = {{0.f, 0.f}, {0.f, 0.f}};
#pragma unroll
    for (int y = 0; y < 2; ++y)
#pragma unroll
      for (int q = 0; q < 4; ++q) {
        const f2 p = (q & 1) ? hp[y][q >> 1].zw : hp[y][q >> 1].xy, qq = (q & 1) ? hq[y][q >> 1].zw : hq[y][q >> 1].xy;
        a1[0] += p; a2[0] = fma2(p, p, a2[0]); a3[0] = fma2(p, wf[y][q], a3[0]);
        a1[1] += qq; a2[1] = fma2(qq, qq, a2[1]); a3[1] = fma2(qq, wf[y][q], a3[1]);
      }
    const float t1 = swap32_add(hadd(a1[0]), hadd(a1[1])), t2 = swap32_add(hadd(a2[0]), hadd(a2[1]));
    float t3 = swap32_add(hadd(a3[0]), hadd(a3[1]));
    const float t12 = row_sum(swap16_add(t1, t2));
    t3 = row_sum(swap16_add(t3, t3));
    auto sw = __builtin_amdgcn_permlane16_swap(asu(t12), asu(t12), false, false);
    const float mean = asf(sw[0]) * (1.0f / 1024.0f);
    float var = __builtin_fmaf(asf(sw[1]), 1.0f / 1024.0f, -mean * mean);
    float mref = 0.f;
    if (__builtin_expect(__builtin_amdgcn_ballot_w64(mean * mean > 16.0f * var) != 0ull, 0)) {
      const float mp = asf(__builtin_amdgcn_readlane(asu(mean), 0)), mq = asf(__builtin_amdgcn_readlane(asu(mean), 32));
      float sp = 0.f, sq = 0.f, dp = 0.f, dq = 0.f;
#pragma unroll
      for (int y = 0; y < 2; ++y)
#pragma unroll
        for (int j = 0; j < 8; ++j) {
          const float e1 = hp[y][j >> 2][j & 3] - mp, e2 = hq[y][j >> 2][j & 3] - mq;
          sp += e1 * e1; sq += e2 * e2;
          dp += e1 * wf[y][j >> 1][j & 1]; dq += e2 * wf[y][j >> 1][j & 1];
        }
      float u1 = swap32_add(sp, sq), u2_ = swap32_add(dp, dq);
      var = row_sum(swap16_add(u1, u1)) * (1.0f / 1024.0f);
      t3 = row_sum(swap16_add(u2_, u2_));
      mref = mean;
    }
    float mb, rs;
    ln_scalars(mean, var, mb, rs);
    const float rr = (t3 + (mref - mb) * wsum) * rs;
    const unsigned rb = pkb(rr, rr) & 0xffffu;
    const unsigned inv = (rb & 0x8000u) ? rb : (rb ^ 0x7fffu);
    const unsigned key = (inv << 16) | (unsigned)(b + 256 * w + 32 * (l & 32));
    if ((l & 31) == 0) __builtin_amdgcn_raw_buffer_store_b32(key, rw, l >> 3, O_KEY + b * 128 + w * 8, SC1);
    SB();
    Tk = now32();
    STAMP(2, 5);
    k0 = __builtin_amdgcn_readlane(key, 0); k1 = __builtin_amdgcn_readlane(key, 32);
    likely = __builtin_amdgcn_ballot_w64(!(rr <= wthr)) != 0ull;
    const float off = -mb * rs;
#pragma unroll
    for (int s = 0; s < 2; ++s) {
      const float rs_ = asf(__builtin_amdgcn_readlane(asu(rs), 32 * s)), of_ = asf(__builtin_amdgcn_readlane(asu(off), 32 * s));
      const f2 r2 = {rs_, rs_}, o2 = {of_, of_};
#pragma unroll
      for (int y = 0; y < 2; ++y)
#pragma unroll
        for (int q = 0; q < 4; ++q) {
          const f4 h4 = s ? hq[y][q >> 1] : hp[y][q >> 1];
          const f2 h = (q & 1) ? h4.zw : h4.xy;
          const f2 ln = fma2(h, r2, o2), o = h + ln;
          lv[s][y][q] = pkb(ln.x, ln.y); hv[s][y][q] = pkb(h.x, h.y); ov[s][y][q] = pkb(o.x, o.y);
        }
    }
  }
  const rsrc_t ro = mkrs(out, 2048 * D_ * 2);
  {
    const unsigned kb = min(k0, k1), ks = max(k0, k1);
    bool open = true, sel0 = false, sel1 = false;
    if (!likely) {
      lds_arrive(L_CT + 4);
      wait_until(Tk + D3U);
      const u4 k4 = __builtin_amdgcn_raw_buffer_load_b128(rw, (l >> 1) * 128 + (l & 1) * 16, O_KEY + ((b >> 3) & 7) * 4096, SC1);
      int n = 0;
#pragma unroll
      for (int j = 0; j < 4; ++j) n += __builtin_popcountll(__builtin_amdgcn_ballot_w64(k4[j] < kb));
      open = n < 8;
    }
    if (open) {
      wait_until(Tk + D3);
      u4 kv[8];
      int nb = 8;
      _Pragma("nounroll") for (int sp = 0; sp < SPIN_CAP; ++sp) {
        MEMB();
#pragma unroll
        for (int j = 0; j < 8; ++j) kv[j] = __builtin_amdgcn_raw_buffer_load_b128(rw, (l >> 1) * 128 + (l & 1) * 16, O_KEY + j * 4096, SC1);
        if (sp == 0 && likely) lds_arrive(L_CT + 4);
        nb = 0;
        unsigned mx = 0;
#pragma unroll
        for (int j = 0; j < 8; ++j) {
#pragma unroll
          for (int q = 0; q < 4; ++q) nb += __builtin_popcountll(__builtin_amdgcn_ballot_w64(kv[j][q] < kb));
          mx = max(mx, umax4(kv[j]));
        }
        if (nb >= 8 || __builtin_amdgcn_ballot_w64(mx == SENT) == 0ull) break;
        __builtin_amdgcn_s_sleep(2);
      }
      if (nb < 8) {
#define PUSH(S, SLOT, KEY)                                                                                          \
  do {                                                                                                              \
    _Pragma("unroll") for (int y = 0; y < 2; ++y) __builtin_amdgcn_raw_buffer_store_b128(lv[S][y], rw, d0 * 2 + 256 * y, O_XS + (SLOT) * 2048, SC1); \
    _Pragma("unroll") for (int y = 0; y < 2; ++y) __builtin_amdgcn_raw_buffer_store_b128(hv[S][y], rw, d0 * 2 + 256 * y, O_XH + (SLOT) * 2048, SC1); \
    if (l == 0) __builtin_amdgcn_raw_buffer_store_b32(KEY, rw, 0, O_XK + (SLOT) * 128, SC1);                         \
  } while (0)
        if (kb == k0) { PUSH(0, nb, kb); sel0 = true; } else { PUSH(1, nb, kb); sel1 = true; }
        STAMP(3, 7);
        int ns = 0;
#pragma unroll
        for (int j = 0; j < 8; ++j)
#pragma unroll
          for (int q = 0; q < 4; ++q) ns += __builtin_popcountll(__builtin_amdgcn_ballot_w64(kv[j][q] < ks));
        if (ns < 8) {
          if (kb == k0) { PUSH(1, ns, ks); sel1 = true; } else { PUSH(0, ns, ks); sel0 = true; }
        }
#undef PUSH
      }
    }
    {
      if (!sel0) {
#pragma unroll
        for (int y = 0; y < 2; ++y) __builtin_amdgcn_raw_buffer_store_b128(ov[0][y], ro, d0 * 2 + 256 * y, (b + 256 * w) * D_ * 2, AUX_OUT);
      }
      if (!sel1) {
#pragma unroll
        for (int y = 0; y < 2; ++y) __builtin_amdgcn_raw_buffer_store_b128(ov[1][y], ro, d0 * 2 + 256 * y, (b + 256 * (w + 4)) * D_ * 2, AUX_OUT);
      }
    }
  }
  STAMP(3, 0);
  if (b < 224) {
    lds_wait(L_CT + 24, 1u);
    const int sr = 4 * (g & 1) + (r >> 2);
    const LAS unsigned char* pw = L + (g >> 1) * 1024 + sr * 128 + (r & 3) * 8;
    u4 wgf[8], wuf[8];
#pragma unroll
    for (int ks = 0; ks < 8; ++ks) {
      const LAS unsigned char* p = pw + L_GU + ks * 4096 + ((w ^ (sr >> 1)) << 5);
      const u2 a0 = trread(p), a1 = trread(p + 2048), c0 = trread(p + 32768), c1 = trread(p + 34816);
      wgf[ks] = u4{a0[0], a0[1], a1[0], a1[1]}; wuf[ks] = u4{c0[0], c0[1], c1[0], c1[1]};
      pin(wgf[ks]); pin(wuf[ks]);
    }
    wait_until(Tk + D4);
    STAMP(3, 1);
    u4 xa[4];
    int np = 0;
    _Pragma("nounroll") for (int sp = 0; sp < SPIN_CAP; ++sp) {
      MEMB();
      ++np;
#pragma unroll
      for (int q = 0; q < 4; ++q) xa[q] = __builtin_amdgcn_raw_buffer_load_b128(rw, (r & 7) * 2048 + (r >> 3) * 256 + g * 16 + q * 64, O_XS + kq * 512, SC1);
      if (__builtin_amdgcn_ballot_w64(max(max(umax4(xa[0]), umax4(xa[1])), max(umax4(xa[2]), umax4(xa[3]))) == SENT) == 0ull) break;
      __builtin_amdgcn_s_sleep(2);
    }
    STAMP(3, 2);
    COUNT(7, 2, 1000 + np);
    f4 g1 = z4, g2 = z4, u1 = z4, u2_ = z4;
#pragma unroll
    for (int q = 0; q < 4; ++q) {
      g1 = mmb(wgf[q], xa[q], g1); u1 = mmb(wuf[q], xa[q], u1);
      g2 = mmb(wgf[q + 4], xa[q], g2); u2_ = mmb(wuf[q + 4], xa[q], u2_);
    }
    u4 cmb;
#pragma unroll
    for (int i = 0; i < 4; ++i) {
      const float gs = g1[i] + dppf<0x128>(g2[i]), us_ = u2_[i] + dppf<0x128>(u1[i]);
      cmb[i] = asu(r < 8 ? gs : us_) & 0xfffffffeu;
    }
    __builtin_amdgcn_raw_buffer_store_b128(cmb, rw, (l >> 3) * BLK + (l & 7) * 16, outbox(b) + 8 * w * BLK, SC1);
    asm volatile("s_nop 1" ::"v"(cmb));
    SB();
    const unsigned Ta = now32() + D5;
    STAMP(3, 3);
    u4 wdf[4][2];
#pragma unroll
    for (int t = 0; t < 4; ++t)
#pragma unroll
      for (int ks = 0; ks < 2; ++ks) {
        const LAS unsigned char* p = pw + L_DN + w * 8192 + ks * 4096 + ((t ^ (sr >> 1)) << 5);
        const u2 a0 = trread(p), a1 = trread(p + 2048);
        wdf[t][ks] = u4{a0[0], a0[1], a1[0], a1[1]};
        pin(wdf[t][ks]);
      }
    wait_until(Ta);
    np = 0;
    _Pragma("nounroll") for (int sp = 0; sp < SPIN_CAP; ++sp) {
      MEMB();
      ++np;
#pragma unroll
      for (int q = 0; q < 4; ++q) xa[q] = __builtin_amdgcn_raw_buffer_load_b128(rw, (l >> 3) * BLK + (l & 7) * 16, outbox((b & ~24) | (q << 3)) + 8 * w * BLK, SC1);
      if (__builtin_amdgcn_ballot_w64((or4(xa[0]) | or4(xa[1]) | or4(xa[2]) | or4(xa[3])) & 1u) == 0ull) break;
      __builtin_amdgcn_s_sleep(1);
    }
    STAMP(3, 4);
    COUNT(7, 3, 1000 + np);
    const f4 sum = (__builtin_bit_cast(f4, xa[0]) + __builtin_bit_cast(f4, xa[1])) + (__builtin_bit_cast(f4, xa[2]) + __builtin_bit_cast(f4, xa[3]));
    const unsigned p0 = pkb(sum.x, sum.y), p1 = pkb(sum.z, sum.w);
    const unsigned got = (unsigned)__builtin_amdgcn_update_dpp(0, (int)(r < 8 ? p1 : p0), 0x128, 0xf, 0xf, true);
    const unsigned gb = r < 8 ? p0 : got, ub = r < 8 ? got : p1;
    const float h0 = blo(gb), h1 = bhi(gb);
    const unsigned sb = pkb(h0 * __builtin_amdgcn_rcpf(1.0f + __expf(-h0)), h1 * __builtin_amdgcn_rcpf(1.0f + __expf(-h1)));
    *(LAS unsigned*)(L + L_EX + (r & 7) * 144 + 32 * w + 8 * g + 4 * (r >> 3)) = pkb(blo(sb) * blo(ub), bhi(sb) * bhi(ub));
    lbar(L_CT + 20, 21u);
    const u4 a0 = *(const LAS u4*)(L + L_EX + (r & 7) * 144 + 16 * g), a1 = *(const LAS u4*)(L + L_EX + (r & 7) * 144 + 64 + 16 * g);
    u4 po[2];
#pragma unroll
    for (int pp = 0; pp < 2; ++pp) {
      const f4 ya = mmb(wdf[2 * pp][1], a1, mmb(wdf[2 * pp][0], a0, z4)), yb = mmb(wdf[2 * pp + 1][1], a1, mmb(wdf[2 * pp + 1][0], a0, z4));
#pragma unroll
      for (int i = 0; i < 4; ++i) po[pp][i] = asu(r < 8 ? ya[i] : yb[i]) & 0xfffffffeu;
      __builtin_amdgcn_raw_buffer_store_b128(po[pp], rw, (r & 7) * BLK + (r >> 3) * 64 + g * 16, outbox(b) + (32 + (2 * w + pp) * 8) * BLK, SC1);
    }
    asm volatile("s_nop 1" ::"v"(po[0]), "v"(po[1]));
  }
  if (WBL2 && b >= 248 && w == 1) { wait_until(Tk + WBL2); asm volatile("buffer_wbl2 sc1" ::: "memory"); }
  if (w != 0) return;
  SB();
  const unsigned Tp = b < 224 ? now32() + D6 : Tk + D4 + D7;
  const int tok = b >> 5, col = (b & 31) * 32 + 4 * (l & 7) + 2 * ((l >> 4) & 1) + (l >> 5);
  if (b >= 224) wait_until(Tk + D4 + 40);
  STAMP(3, 5);
  {
    unsigned key, lnw, hw;
    _Pragma("nounroll") for (int sp = 0; sp < SPIN_CAP; ++sp) {
      MEMB();
      key = __builtin_amdgcn_raw_buffer_load_b32(rw, 0, O_XK + tok * 128, SC1);
      lnw = __builtin_amdgcn_raw_buffer_load_b32(rw, (col >> 1) * 4, O_XS + tok * 2048, SC1);
      hw = __builtin_amdgcn_raw_buffer_load_b32(rw, (col >> 1) * 4, O_XH + tok * 2048, SC1);
      if (__builtin_amdgcn_ballot_w64(umax3(key, lnw, hw) == SENT) == 0ull) break;
      __builtin_amdgcn_s_sleep(1);
    }
    const unsigned inv = key >> 16;
    const float rv = blo((inv & 0x8000u) ? inv : (inv ^ 0x7fffu));
    float gate = rbf(__builtin_amdgcn_rcpf(1.0f + __expf(-rv)));
    float lnf = (col & 1) ? bhi(lnw) : blo(lnw), hf = (col & 1) ? bhi(hw) : blo(hw);
    pin(gate); pin(lnf); pin(hf);
    wait_until(Tp);
    u4 pp[7];
    int np = 0;
    _Pragma("nounroll") for (int sp = 0; sp < SPIN_CAP; ++sp) {
      MEMB();
      ++np;
      unsigned mx = 0;
#pragma unroll
      for (int i = 0; i < 7; ++i) pp[i] = __builtin_amdgcn_raw_buffer_load_b128(rw, (l >> 3) * 1024 + (l & 7) * 16, outbox((b & 24) + 32 * i) + (32 + (b & 7) * 8 + tok) * BLK, SC1);
#pragma unroll
      for (int i = 0; i < 7; ++i) mx |= or4(pp[i]);
      if (__builtin_amdgcn_ballot_w64(mx & 1u) == 0ull) break;
      __builtin_amdgcn_s_sleep(1);
    }
    COUNT(7, 4, 1000 + np);
    f4 ys = __builtin_bit_cast(f4, pp[0]);
#pragma unroll
    for (int i = 1; i < 7; ++i) ys += __builtin_bit_cast(f4, pp[i]);
    const float sa = swap32_add(ys.x, ys.y), sb = swap32_add(ys.z, ys.w);
    float y = swap16_add(sa, sb);
    y += dppf<0x128>(y);
    const float o = hf + rbf(lnf + rbf(rbf(y) * gate));
    if (r < 8) __builtin_amdgcn_raw_buffer_store_b16((us)(pkb(o, o) & 0xffffu), ro, col * 2 + (key & 2047u) * (D_ * 2), 0, AUX_OUT);
    STAMP(3, 6);
    DUMP();
  }
}

extern "C" void kernel_launch(void* const* d_in, const int* in_sizes, int n_in, void* d_out, int out_size, void* d_ws, size_t ws_size,
                              hipStream_t stream) {
  Args a;
  a.x = (const us*)d_in[0]; a.wrt = (const us*)d_in[4]; a.wr = (const us*)d_in[1]; a.wi = (const us*)d_in[2];
  a.wg = (const us*)d_in[5]; a.wu = (const us*)d_in[6]; a.wd = (const us*)d_in[7];
  a.out = (us*)d_out; a.ws = (unsigned char*)d_ws + (size_t)WS_MB * 1048576;
  fused_kernel<<<256, CPF >= 2 ? 448 : 384, 0, stream>>>(a);
}
```
